```python
import torch

def _softmax(x, axis=-1):
    return torch.softmax(x, dim=axis)
def _gelu(x):
    return torch.nn.functional.gelu(x, approximate="tanh")
def _silu(x):
    return torch.nn.functional.silu(x)
def _sigmoid(x):
    return torch.sigmoid(x)
def _layernorm(x, axis=-1, eps=1e-5):
    m = x.mean(dim=axis, keepdim=True)
    v = x.var(dim=axis, keepdim=True, unbiased=False)
    return (x - m) / torch.sqrt(v + eps)
def _scatter_add(tgt, idx, src, axis=0):
    return tgt.clone().index_add_(axis, idx, src)
def _segment_sum(src, seg, n):
    out = torch.zeros((n,) + src.shape[1:], dtype=src.dtype)
    return out.index_add_(0, seg, src)
def _pad(x, pw):
    flat = tuple(v for pair in reversed(pw) for v in pair)
    return torch.nn.functional.pad(x, flat)

kernel_name = 'parallel_sum__ssm_conv_s4d__mixer_channel_mlp__prompt'

def setup_inputs():

    _w = (lambda a: (lambda t: t.to(torch.bfloat16) if t.is_floating_point() else t)(torch.as_tensor(a)))
    def _A_setup():
        Bsz, L, D, N = 1, 2048, 512, 128
        return {
            "u": _w((lambda *s: torch.randn(*s, dtype=torch.bfloat16) * 0.05)(Bsz, L, D)),
            "log_A_real": _w((lambda *s: torch.randn(*s, dtype=torch.bfloat16) * 3.2)(D, N) * 0.5),
            "A_imag": _w((lambda *s: torch.randn(*s, dtype=torch.bfloat16) * 0.05)(D, N) * 3.14),
            "C_real": _w((lambda *s: torch.randn(*s, dtype=torch.bfloat16) * 0.1)(D, N) / N ** 0.5),
            "C_imag": _w((lambda *s: torch.randn(*s, dtype=torch.bfloat16) * 0.2)(D, N) / N ** 0.5),
            "log_dt": _w((lambda *s: torch.randn(*s, dtype=torch.bfloat16) * 0.05)(D) * 0.5 - 4.0),
            "D_skip": _w((lambda *s: torch.randn(*s, dtype=torch.bfloat16) * 0.05)(D)),
        }
    _A_in = _A_setup()

    def _B_setup():
        B, S, D, F = 1, 2048, 512, 1376
        return {
            "x": _w((lambda *s: torch.randn(*s, dtype=torch.bfloat16) * 0.05)(B, S, D)),
            "w1": _w((lambda *s: torch.randn(*s, dtype=torch.bfloat16) * 1.6)(D, F) * (D ** -0.5)),
            "b1": _w((lambda *s: torch.randn(*s, dtype=torch.bfloat16) * 0.8)(F)),
            "w2": _w((lambda *s: torch.randn(*s, dtype=torch.bfloat16) * 0.05)(F, D) * (F ** -0.5)),
            "b2": _w((lambda *s: torch.randn(*s, dtype=torch.bfloat16) * 0.05)(D)),
        }
    _B_in = _B_setup()

    return {
        "A_u": _A_in["u"],
        "A_log_A_real": _A_in["log_A_real"],
        "A_A_imag": _A_in["A_imag"],
        "A_C_real": _A_in["C_real"],
        "A_C_imag": _A_in["C_imag"],
        "A_log_dt": _A_in["log_dt"],
        "A_D_skip": _A_in["D_skip"],
        "B_x": _B_in["x"],
        "B_w1": _B_in["w1"],
        "B_b1": _B_in["b1"],
        "B_w2": _B_in["w2"],
        "B_b2": _B_in["b2"],
    }

def reference(A_u, A_log_A_real, A_A_imag, A_C_real, A_C_imag, A_log_dt, A_D_skip, B_x, B_w1, B_b1, B_w2, B_b2):
    def _A_ref(u, log_A_real, A_imag, C_real, C_imag, log_dt, D_skip):
        L = u.shape[1]
        dt = torch.exp(log_dt)
        A = -torch.exp(log_A_real) + 1j * A_imag
        C = (C_real + 1j * C_imag) * dt[:, None]
        pos = (lambda x, dt: x.to(dt))(torch.arange(L), log_dt.dtype)
        P = torch.exp((A * dt[:, None])[:, :, None] * pos[None, None, :])
        K = 2.0 * torch.einsum("dn,dnl->dl", C, P).real
        L2 = 2 * L
        u_t = (lambda x, *d: x.permute(*d))(u, 0, 2, 1)
        Kf = (lambda x, *a, **k: torch.fft.rfft(x.float(), *a, **k))(K, L2)
        Uf = (lambda x, *a, **k: torch.fft.rfft(x.float(), *a, **k))(u_t, L2)
        y = (lambda x, *a, **k: torch.fft.irfft(x, *a, **k).to(torch.bfloat16))(Uf * Kf[None, :, :], L2)[:, :, :L]
        y = (lambda x, *d: x.permute(*d))(y, 0, 2, 1)
        return y + u * D_skip[None, None, :]

    def _B_ref(x, w1, b1, w2, b2):
        y = _layernorm(x, axis=-1)
        h = _gelu(torch.einsum("bsd,df->bsf", y, w1) + b1)
        return torch.einsum("bsf,fd->bsd", h, w2) + b2

    x = A_u
    xn = _layernorm(x, axis=-1)
    return x + _A_ref(xn, A_log_A_real, A_A_imag, A_C_real, A_C_imag, A_log_dt, A_D_skip) + _B_ref(xn, B_w1, B_b1, B_w2, B_b2)
```

```cpp
#include <hip/hip_runtime.h>
#include <hip/hip_bf16.h>
#include <stdint.h>

#define SPLIT_LAUNCH 1

namespace {

typedef float f2 __attribute__((ext_vector_type(2)));
typedef float f4 __attribute__((ext_vector_type(4)));
typedef unsigned u2 __attribute__((ext_vector_type(2)));
typedef unsigned u4 __attribute__((ext_vector_type(4)));
typedef short s4 __attribute__((ext_vector_type(4)));
typedef _Float16 h2 __attribute__((ext_vector_type(2)));
typedef _Float16 h8 __attribute__((ext_vector_type(8)));
typedef __bf16 b2 __attribute__((ext_vector_type(2)));
typedef __bf16 b8 __attribute__((ext_vector_type(8)));
typedef unsigned long long u64;
#define LAS __attribute__((address_space(3)))
#define GAS __attribute__((address_space(1)))
typedef GAS u64 gu64;

constexpr int SEQ = 2048, DM = 512, NST = 128, FF = 1376;

constexpr unsigned WS_EPOCH = 0;
constexpr unsigned WS_XNFLAG = 4096;
constexpr unsigned WS_HFLAG = 4096 + 2048;
constexpr unsigned WS_AFLAG = 4096 + 4096;
constexpr unsigned WS_STATS = 16384;
constexpr unsigned WS_CS = 32768;
constexpr unsigned WS_XNB = 65536;
constexpr unsigned WS_AT = WS_XNB + 2097152;
constexpr unsigned WS_H = WS_AT + 2097152;
constexpr unsigned WS_END = WS_H + 128 * 43 * 1024;

constexpr int LDS_BYTES = 81920;

struct Params {
  const char* x;
  const unsigned short* logA;
  const unsigned short* Aim;
  const unsigned short* Cre;
  const unsigned short* Cim;
  const unsigned short* logdt;
  const unsigned short* dskip;
  const char* w1;
  const char* b1;
  const char* w2;
  const char* b2;
  char* out;
  char* ws;
  int mode;
  int pad;
};

__device__ __forceinline__ float bf_lo(unsigned u) { return __builtin_bit_cast(float, u << 16); }
__device__ __forceinline__ float bf_hi(unsigned u) { return __builtin_bit_cast(float, u & 0xffff0000u); }
__device__ __forceinline__ float bf_us(unsigned short u) { return __builtin_bit_cast(float, (unsigned)u << 16); }
__device__ __forceinline__ unsigned pk_bf(float a, float b) { b2 v = {(__bf16)a, (__bf16)b}; return __builtin_bit_cast(unsigned, v); }
__device__ __forceinline__ unsigned pk_h(float a, float b) { h2 v = {(_Float16)a, (_Float16)b}; return __builtin_bit_cast(unsigned, v); }
__device__ __forceinline__ float bfr(float a) { return bf_lo(pk_bf(a, a)); }
__device__ __forceinline__ float asf(unsigned u) { return __builtin_bit_cast(float, u); }
__device__ __forceinline__ unsigned asu(float u) { return __builtin_bit_cast(unsigned, u); }

template <int CTRL>
__device__ __forceinline__ float dpp_add(float x) {
  return x + asf((unsigned)__builtin_amdgcn_update_dpp(0, (int)asu(x), CTRL, 0xf, 0xf, true));
}
__device__ __forceinline__ float sum16(float x) {
  x = dpp_add<0x128>(x); x = dpp_add<0x124>(x); x = dpp_add<0x122>(x); x = dpp_add<0x121>(x);
  return x;
}
__device__ __forceinline__ float swap16_add(float a, float b) {
  auto r = __builtin_amdgcn_permlane16_swap(asu(a), asu(b), false, false);
  return asf(r[0]) + asf(r[1]);
}
__device__ __forceinline__ float swap32_add(float a, float b) {
  auto r = __builtin_amdgcn_permlane32_swap(asu(a), asu(b), false, false);
  return asf(r[0]) + asf(r[1]);
}
__device__ __forceinline__ float sum32(float x) { x = sum16(x); return swap16_add(x, x); }
__device__ __forceinline__ float sum64(float x) { x = sum32(x); return swap32_add(x, x); }

__device__ __forceinline__ f2 cmul(f2 a, f2 b) {
  f2 t, r;
  asm("v_pk_mul_f32 %0, %1, %2 op_sel:[0,0] op_sel_hi:[0,1]" : "=v"(t) : "v"(a), "v"(b));
  asm("v_pk_fma_f32 %0, %1, %2, %3 op_sel:[1,1,0] op_sel_hi:[1,0,1] neg_lo:[0,1,0]" : "=v"(r) : "v"(a), "v"(b), "v"(t));
  return r;
}

#define BAR_LDS() asm volatile("s_waitcnt lgkmcnt(0)\n\ts_barrier" ::: "memory")
#define WAIT_VM(n) asm volatile("s_waitcnt vmcnt(" #n ")" ::: "memory")

__device__ __forceinline__ u64 ld_flag(const char* p) {
  return __hip_atomic_load((gu64*)p, __ATOMIC_RELAXED, __HIP_MEMORY_SCOPE_AGENT);
}
__device__ __forceinline__ void st_flag(char* p, u64 v) {
  __hip_atomic_store((gu64*)p, v, __ATOMIC_RELAXED, __HIP_MEMORY_SCOPE_AGENT);
}
__device__ __forceinline__ void poll_flags(const char* p, bool active, u64 tag) {
  for (int spins = 0; spins < (1 << 20); ++spins) {
    u64 v = tag;
    if (active) v = ld_flag(p);
    if (__all(v == tag)) break;
    __builtin_amdgcn_s_sleep(1);
  }
}

template <typename T>
__device__ __forceinline__ LAS T* lds_at(unsigned off) {
  extern __shared__ __attribute__((aligned(16))) char smem[];
  return (LAS T*)((LAS char*)smem + off);
}
__device__ __forceinline__ unsigned lds_base() {
  extern __shared__ __attribute__((aligned(16))) char smem[];
  return (unsigned)(size_t)(LAS char*)smem;
}
__device__ __forceinline__ s4 tr_read(unsigned off) {
  return __builtin_amdgcn_ds_read_tr16_b64_v4i16(lds_at<s4>(off));
}

constexpr unsigned S_STAGE = 0;
constexpr unsigned S_CSP = 8704;
constexpr unsigned S_TB = 0;
constexpr unsigned S_TA = 43520;
constexpr unsigned S_R = 70720;
constexpr unsigned S_MISC = 79168;
constexpr unsigned S_CP = 0;
constexpr unsigned S_U = 35328;
constexpr unsigned S_Y = 0;

__device__ __forceinline__ void s_layernorm(const Params& p, int sb, u64 tag, bool fused) {
  const int tid = threadIdx.x, w = tid >> 6, l = tid & 63, c = l & 31;
  const int tl = 2 * w + (l >> 5);
  const int t = 8 * sb + tl;
  const u4* xr = (const u4*)(p.x + (size_t)t * 1024);
  const u4 xa = xr[c], xb = xr[32 + c];
  u4 wa = {0, 0, 0, 0}, wb = {0, 0, 0, 0};
  const bool do_cs = sb < FF / 8;
  if (do_cs) {
    wa = *(const u4*)(p.w1 + (size_t)(2 * tid) * (FF * 2) + sb * 16);
    wb = *(const u4*)(p.w1 + (size_t)(2 * tid + 1) * (FF * 2) + sb * 16);
  }
  float v[16];
#pragma unroll
  for (int i = 0; i < 4; ++i) {
    v[2 * i] = bf_lo(xa[i]); v[2 * i + 1] = bf_hi(xa[i]);
    v[8 + 2 * i] = bf_lo(xb[i]); v[8 + 2 * i + 1] = bf_hi(xb[i]);
  }
  float s = 0.f;
#pragma unroll
  for (int i = 0; i < 16; ++i) s += v[i];
  const float mean = sum32(s) * (1.f / 512.f);
  float ss = 0.f;
#pragma unroll
  for (int i = 0; i < 16; ++i) { const float d = v[i] - mean; ss = fmaf(d, d, ss); }
  const float var = sum32(ss) * (1.f / 512.f);
  const float m = bfr(mean);
  const float sd = bfr(__builtin_amdgcn_sqrtf(bfr(bfr(var) + 1e-5f)));
  const float r1 = __builtin_amdgcn_rcpf(sd);
  float s2 = 0.f, q2 = 0.f;
  unsigned hx[8];
#pragma unroll
  for (int i = 0; i < 8; ++i) {
    const unsigned cc = pk_bf(v[2 * i] - m, v[2 * i + 1] - m);
    const unsigned nn = pk_bf(bf_lo(cc) * r1, bf_hi(cc) * r1);
    const float a = bf_lo(nn), b = bf_hi(nn);
    s2 += a; s2 += b; q2 = fmaf(a, a, q2); q2 = fmaf(b, b, q2);
    hx[i] = pk_h(a, b);
  }
  *lds_at<u4>(S_STAGE + tl * 1088 + c * 16) = u4{hx[0], hx[1], hx[2], hx[3]};
  *lds_at<u4>(S_STAGE + tl * 1088 + 512 + c * 16) = u4{hx[4], hx[5], hx[6], hx[7]};
  const float mean2 = sum32(s2) * (1.f / 512.f);
  const float var2 = fmaxf(sum32(q2) * (1.f / 512.f) - mean2 * mean2, 0.f);
  const float m2 = bfr(mean2);
  const float sd2 = bfr(__builtin_amdgcn_sqrtf(bfr(bfr(var2) + 1e-5f)));
  const float mu = fmaf(m2, sd, m);
  const float rho = __builtin_amdgcn_rcpf(sd * sd2);
  {
    float cs[8];
#pragma unroll
    for (int i = 0; i < 4; ++i) {
      cs[2 * i] = bf_lo(wa[i]) + bf_lo(wb[i]);
      cs[2 * i + 1] = bf_hi(wa[i]) + bf_hi(wb[i]);
    }
    float d[4], e[2];
#pragma unroll
    for (int j = 0; j < 4; ++j) d[j] = swap32_add(cs[j], cs[j + 4]);
#pragma unroll
    for (int j = 0; j < 2; ++j) e[j] = sum16(swap16_add(d[j], d[j + 2]));
    if ((l & 15) == 0) *lds_at<f2>(S_CSP + w * 32 + (l >> 4) * 8) = f2{e[0], e[1]};
  }
  BAR_LDS();
  const auto rs = __builtin_amdgcn_make_buffer_rsrc((void*)p.ws, 0, WS_END, 0x00020000);
#pragma unroll
  for (int ps = 0; ps < 2; ++ps) {
    const int cb = 128 * w + 64 * ps + 16 * (l >> 4);
    const int q = (l & 15) >> 2, pp = l & 3;
    const s4 lo = tr_read(S_STAGE + q * 1088 + (cb + 4 * pp) * 2);
    const s4 hi = tr_read(S_STAGE + (4 + q) * 1088 + (cb + 4 * pp) * 2);
    const u2 a = __builtin_bit_cast(u2, lo), b = __builtin_bit_cast(u2, hi);
    __builtin_amdgcn_raw_buffer_store_b128(u4{a[0], a[1], b[0], b[1]}, rs,
                                           WS_XNB + sb * 8192 + (128 * w + 64 * ps + l) * 16, 0, 16);
  }
  if (c == 0) __builtin_amdgcn_raw_buffer_store_b64(u2{asu(mu), asu(rho)}, rs, WS_STATS + t * 8, 0, 16);
  if (do_cs && tid < 8) {
    const float a = *lds_at<float>(S_CSP + tid * 4), b = *lds_at<float>(S_CSP + 32 + tid * 4);
    const float cc = *lds_at<float>(S_CSP + 64 + tid * 4), d = *lds_at<float>(S_CSP + 96 + tid * 4);
    __builtin_amdgcn_raw_buffer_store_b32(asu((a + b) + (cc + d)), rs, WS_CS + (8 * sb + tid) * 4, 0, 16);
  }
  WAIT_VM(0);
  BAR_LDS();
  if (fused && tid == 0) st_flag(p.ws + WS_XNFLAG + sb * 8, tag);
}

__device__ __forceinline__ constexpr int row_of(int e) {
  return e < 16 ? e : (e < 32 ? 16 + (e - 16) / 2 : (e < 64 ? 24 + (e - 32) / 4 : 32 + (e - 64) / 8));
}

__device__ __forceinline__ void s_conv(const Params& p, int sb, u64 tag, bool fused) {
  const int tid = threadIdx.x, w = __builtin_amdgcn_readfirstlane(tid >> 6), l = tid & 63;
  const int chn = w >> 1, hf = w & 1;
  const int pair = 32 * (sb & 7) + (sb >> 3);
  const int ch = 2 * pair + chn;
  const int j16 = l & 15, g = l >> 4;

  const int pi = ch * NST + 64 * hf + l;
  const float dt = bfr(expf(bf_us(p.logdt[ch])));
  const float ar = -bfr(expf(bf_us(p.logA[pi])));
  const float zr = ar * dt, zi = bf_us(p.Aim[pi]) * dt;
  const float cr = bf_us(p.Cre[pi]) * dt, ci = bf_us(p.Cim[pi]) * dt;
  const float bound = sum64(fabsf(cr) + fabsf(ci));
  const int eb = __builtin_amdgcn_frexp_expf(bound);
  const float sA = __builtin_amdgcn_ldexpf(1.f, 8 - eb), isA = __builtin_amdgcn_ldexpf(1.f, eb - 8);
  const f2 C = {cr * sA, ci * sA};
  const float a2 = zr * 1.44269504088896341f, b2r = zi * 0.15915494309189535f;
  auto direct = [&](float e) -> f2 {
    const float mag = __builtin_amdgcn_exp2f(a2 * e);
    const float fr = __builtin_amdgcn_fractf(b2r * e);
    return f2{mag * __builtin_amdgcn_cosf(fr), mag * __builtin_amdgcn_sinf(fr)};
  };
  const unsigned tb = S_TB + w * (40 * 272) + l * 4, ta = S_TA + w * (25 * 272) + l * 4;
  auto putB = [&](int row, f2 v) { *lds_at<unsigned>(tb + row * 272) = pk_h(v.x, v.y); };
  auto putA = [&](int row, f2 v) { *lds_at<unsigned>(ta + row * 272) = pk_h(v.x, -v.y); };

  f2 P[40];
  P[0] = f2{1.f, 0.f};
  P[1] = direct(1.f); P[2] = direct(2.f); P[4] = direct(4.f); P[8] = direct(8.f);
  P[row_of(16)] = direct(16.f); P[row_of(32)] = direct(32.f); P[row_of(64)] = direct(64.f);
  const f2 P128 = direct(128.f), P256 = direct(256.f), P512 = direct(512.f), P1024 = direct(1024.f);
  P[3] = cmul(P[2], P[1]); P[5] = cmul(P[4], P[1]); P[6] = cmul(P[4], P[2]); P[7] = cmul(P[4], P[3]);
#pragma unroll
  for (int e = 9; e < 16; ++e) P[e] = cmul(P[8], P[e - 8]);
#pragma unroll
  for (int e = 18; e < 32; e += 2) P[row_of(e)] = cmul(P[row_of(16)], P[row_of(e - 16)]);
#pragma unroll
  for (int e = 36; e < 64; e += 4) P[row_of(e)] = cmul(P[row_of(32)], P[row_of(e - 32)]);
#pragma unroll
  for (int e = 72; e < 128; e += 8) P[row_of(e)] = cmul(P[row_of(64)], P[row_of(e - 64)]);
#pragma unroll
  for (int r = 0; r < 40; ++r) putB(r, P[r]);
  putA(0, C);
#pragma unroll
  for (int i = 1; i < 8; ++i) putA(i, cmul(C, P[row_of(16 * i)]));
  const f2 Q128 = cmul(C, P128);
  putA(8, Q128);
#pragma unroll
  for (int i = 1; i < 8; ++i) putA(8 + i, cmul(Q128, P[row_of(16 * i)]));

  const unsigned rb = S_TB + w * (40 * 272) + g * 16, ra = S_TA + w * (25 * 272) + g * 16;
  auto seg_mfma = [&](unsigned arow, unsigned brow) -> f4 {
    f4 acc = {0.f, 0.f, 0.f, 0.f};
#pragma unroll
    for (int s = 0; s < 4; ++s) {
      const h8 a = *lds_at<h8>(ra + arow * 272 + s * 64);
      const h8 b = *lds_at<h8>(rb + brow * 272 + s * 64);
      acc = __builtin_amdgcn_mfma_f32_16x16x32_f16(a, b, acc, 0, 0, 0);
    }
    return acc;
  };
  auto row_rt = [&](int e) -> unsigned {
    return e < 16 ? e : (e < 32 ? 16 + ((e - 16) >> 1) : (e < 64 ? 24 + ((e - 32) >> 2) : 32 + ((e - 64) >> 3)));
  };
  f4 D1 = seg_mfma(j16, j16);
  {
    const f2 P32 = P[row_of(32)], P64 = P[row_of(64)], P96 = P[row_of(96)];
    const f2 P160 = cmul(P128, P32), P192 = cmul(P128, P64), P224 = cmul(P128, P96);
    const f2 Q256 = cmul(C, P256);
    putA(0, Q256); putA(1, cmul(Q256, P32)); putA(2, cmul(Q256, P64)); putA(3, cmul(Q256, P96));
    putA(4, cmul(Q256, P128)); putA(5, cmul(Q256, P160)); putA(6, cmul(Q256, P192)); putA(7, cmul(Q256, P224));
    const f2 P320 = cmul(P256, P64), P384 = cmul(P256, P128), P448 = cmul(P256, P192);
    const f2 Q512 = cmul(C, P512);
    putA(8, Q512); putA(9, cmul(Q512, P64)); putA(10, cmul(Q512, P128)); putA(11, cmul(Q512, P192));
    putA(12, cmul(Q512, P256)); putA(13, cmul(Q512, P320)); putA(14, cmul(Q512, P384)); putA(15, cmul(Q512, P448));
    const f2 P640 = cmul(P512, P128), P768 = cmul(P512, P256), P896 = cmul(P512, P384);
    const f2 Q1024 = cmul(C, P1024);
    putA(16, Q1024); putA(17, cmul(Q1024, P128)); putA(18, cmul(Q1024, P256)); putA(19, cmul(Q1024, P384));
    putA(20, cmul(Q1024, P512)); putA(21, cmul(Q1024, P640)); putA(22, cmul(Q1024, P768)); putA(23, cmul(Q1024, P896));
    putA(24, cmul(Q1024, P1024));
  }
  const int i8 = j16 < 8 ? j16 : 7;
  f4 D2 = seg_mfma(i8, row_rt(2 * j16));
  f4 D4 = seg_mfma(8 + i8, row_rt(4 * j16));
  f4 D8 = seg_mfma(16 + (j16 < 9 ? j16 : 8), row_rt(8 * j16));
  D1 *= isA; D2 *= isA; D4 *= isA; D8 *= isA;
  {
    const unsigned ex = S_TA + w * (25 * 272);
    *lds_at<f4>(ex + l * 16) = hf ? D1 : D2;
    *lds_at<f4>(ex + 1024 + l * 16) = hf ? D8 : D4;
    if (l == 0) *lds_at<float>(S_MISC + w * 4) = bound;
  }
  {
    if (tid < 2 * 64) {
      const int c2 = tid >> 6, e = tid & 63;
      const int ee = e < 9 ? e : 2057 + (e - 9);
      if (ee < 2112) *lds_at<unsigned short>(S_R + c2 * 4224 + ee * 2) = 0;
    }
  }
  BAR_LDS();
  float invscale;
  {
    const unsigned ex = S_TA + (w ^ 1) * (25 * 272);
    const f4 o0 = *lds_at<f4>(ex + l * 16), o1 = *lds_at<f4>(ex + 1024 + l * 16);
    const float btot = 2.f * (bound + *lds_at<float>(S_MISC + (w ^ 1) * 4));
    const int et = __builtin_amdgcn_frexp_expf(btot);
    const float sc = __builtin_amdgcn_ldexpf(2.f, 15 - et);
    invscale = __builtin_amdgcn_ldexpf(1.f, et - 15);
    const unsigned rbase = S_R + chn * 4224 + (2048 + 8) * 2;
    auto put = [&](int lag, float val) { *lds_at<_Float16>(rbase - lag * 2) = (_Float16)val; };
    if (hf == 0) {
      const f4 K1 = (D1 + o0) * sc, K8 = (D8 + o1) * sc;
#pragma unroll
      for (int r = 0; r < 4; ++r) put(16 * (4 * g + r) + j16, K1[r]);
#pragma unroll
      for (int r = 0; r < 4; ++r) {
        const int i = 4 * g + r, m = 128 + 16 * i + j16, pp = 8 * m;
        const bool rowok = i < 8 || (i == 8 && j16 == 0);
#pragma unroll
        for (int d = -4; d <= 4; ++d) {
          bool ok = rowok && ((d > -4 && d < 4) || !(j16 & 1));
          if (d < -2) ok = ok && m != 128;
          if (d >= 0) ok = ok && m != 256;
          if (ok) put(pp + d, K8[r]);
        }
      }
    } else {
      const f4 K2 = (D2 + o0) * sc, K4 = (D4 + o1) * sc;
#pragma unroll
      for (int r = 0; r < 4; ++r) {
        const int i = 4 * g + r, m = 128 + 16 * i + j16;
        const bool rowok = i < 8;
#pragma unroll
        for (int d = -1; d <= 1; ++d) {
          bool ok = rowok && (d == 0 || !(j16 & 1));
          if (d < 0) ok = ok && m != 128;
          if (ok) put(2 * m + d, K2[r]);
        }
#pragma unroll
        for (int d = -2; d <= 2; ++d) {
          bool ok = rowok && ((d > -2 && d < 2) || !(j16 & 1));
          if (d < -1) ok = ok && m != 128;
          if (ok) put(4 * m + d, K4[r]);
        }
      }
    }
    if (l == 0 && hf == 0) *lds_at<float>(S_MISC + 16 + chn * 4) = invscale;
  }
  BAR_LDS();
#pragma unroll
  for (int it = 0; it < 3; ++it) {
    int c2, Y;
    if (it < 2) { c2 = it; Y = tid; } else { c2 = tid >> 3; Y = 256 + (tid & 7); }
    if (it < 2 || (tid < 16 && (tid & 7) < 5)) {
      const u4 lo = *lds_at<u4>(S_R + c2 * 4224 + Y * 16), hi = *lds_at<u4>(S_R + c2 * 4224 + Y * 16 + 16);
      const unsigned o2 = __builtin_amdgcn_alignbit(lo[3], lo[2], 16), o3 = __builtin_amdgcn_alignbit(hi[0], lo[3], 16);
      const unsigned o4 = __builtin_amdgcn_alignbit(hi[1], hi[0], 16), o5 = __builtin_amdgcn_alignbit(hi[2], hi[1], 16);
      const unsigned o6 = __builtin_amdgcn_alignbit(hi[3], hi[2], 16);
      const unsigned dst = S_CP + c2 * 17664 + Y * 16;
      *lds_at<u4>(dst) = hi;
      *lds_at<u4>(dst + 4416) = u4{o3, o4, o5, o6};
      *lds_at<u4>(dst + 2 * 4416) = u4{lo[3], hi[0], hi[1], hi[2]};
      *lds_at<u4>(dst + 3 * 4416) = u4{o2, o3, o4, o5};
    }
  }
  for (int i = tid; i < 320; i += 256) {
    const int c2 = i / 160, k = i - c2 * 160;
    *lds_at<u4>(S_U + c2 * 7680 + k * 16) = u4{0, 0, 0, 0};
  }
  if (fused) poll_flags(p.ws + WS_XNFLAG + tid * 8, true, tag);
  {
    const auto rs = __builtin_amdgcn_make_buffer_rsrc((void*)p.ws, 0, WS_END, 0x00020000);
    const unsigned src = WS_XNB + tid * 8192 + pair * 32;
    const u4 ua = __builtin_amdgcn_raw_buffer_load_b128(rs, src, 0, 16);
    const u4 ub = __builtin_amdgcn_raw_buffer_load_b128(rs, src + 16, 0, 16);
    const unsigned dst = S_U + (16 + (tid >> 3)) * 160 + (tid & 7) * 16;
    *lds_at<u4>(dst) = ua;
    *lds_at<u4>(dst + 7680) = ub;
  }
  BAR_LDS();
  f4 acc[2][4];
#pragma unroll
  for (int ct = 0; ct < 2; ++ct)
#pragma unroll
    for (int r = 0; r < 4; ++r) acc[ct][r] = f4{0.f, 0.f, 0.f, 0.f};
  {
    const int a = j16 >> 2, b = j16 & 3;
    const unsigned abase = S_CP + chn * 17664 + b * 4416 + (2048 - 16 * 127 - 4 * a + 8 * g) * 2;
    const unsigned ubase = S_U + chn * 7680 + j16 * 160 + g * 16;
    auto afrag = [&](int ww) -> h8 {
      const u2 lo = *lds_at<u2>(abase + (127 - ww) * 32), hi = *lds_at<u2>(abase + (127 - ww) * 32 + 8);
      return __builtin_bit_cast(h8, u4{lo[0], lo[1], hi[0], hi[1]});
    };
    auto bfrag = [&](int ct, int q) -> h8 {
      const int off = (16 + 16 * ct) * 160 + ((q & 1) ? -160 * ((q - 1) / 2) : -160 * (q / 2) + 64);
      return *lds_at<h8>(ubase + off);
    };
    if (hf == 0) {
      h8 a0, a1;
#pragma unroll
      for (int q = 0; q < 24; ++q) {
        const h8 a2f = afrag(2 * q), a3f = afrag(2 * q + 1);
#pragma unroll
        for (int ct = 0; ct < 2; ++ct) {
          const h8 b = bfrag(ct, q);
          if (q > 0) {
            acc[ct][0] = __builtin_amdgcn_mfma_f32_16x16x32_f16(a0, b, acc[ct][0], 0, 0, 0);
            acc[ct][1] = __builtin_amdgcn_mfma_f32_16x16x32_f16(a1, b, acc[ct][1], 0, 0, 0);
          }
          acc[ct][2] = __builtin_amdgcn_mfma_f32_16x16x32_f16(a2f, b, acc[ct][2], 0, 0, 0);
          acc[ct][3] = __builtin_amdgcn_mfma_f32_16x16x32_f16(a3f, b, acc[ct][3], 0, 0, 0);
        }
        a0 = a2f; a1 = a3f;
      }
    } else {
      h8 a0 = afrag(46), a1 = afrag(47);
#pragma unroll
      for (int q = 24; q < 64; ++q) {
        const h8 a2f = afrag(2 * q), a3f = afrag(2 * q + 1);
#pragma unroll
        for (int ct = 0; ct < 2; ++ct) {
          if (ct == 1 || q < 32) {
            const h8 b = bfrag(ct, q);
            acc[ct][0] = __builtin_amdgcn_mfma_f32_16x16x32_f16(a0, b, acc[ct][0], 0, 0, 0);
            acc[ct][1] = __builtin_amdgcn_mfma_f32_16x16x32_f16(a1, b, acc[ct][1], 0, 0, 0);
            acc[ct][2] = __builtin_amdgcn_mfma_f32_16x16x32_f16(a2f, b, acc[ct][2], 0, 0, 0);
            acc[ct][3] = __builtin_amdgcn_mfma_f32_16x16x32_f16(a3f, b, acc[ct][3], 0, 0, 0);
          }
        }
        a0 = a2f; a1 = a3f;
      }
    }
  }
  BAR_LDS();
#pragma unroll
  for (int ct = 0; ct < 2; ++ct)
#pragma unroll
    for (int r = 0; r < 4; ++r)
      *lds_at<f4>(S_Y + w * 8704 + (16 * ct + j16) * 272 + (16 * r + 4 * g) * 4) = acc[ct][r];
  BAR_LDS();
  {
    const int I = tid >> 3, j0 = 8 * (tid & 7);
    unsigned res[2][4];
#pragma unroll
    for (int c2 = 0; c2 < 2; ++c2) {
      const unsigned y0 = S_Y + (2 * c2) * 8704 + I * 272 + j0 * 4;
      const f4 ya = *lds_at<f4>(y0) + *lds_at<f4>(y0 + 8704), yb = *lds_at<f4>(y0 + 16) + *lds_at<f4>(y0 + 8704 + 16);
      const h8 uu = *lds_at<h8>(S_U + c2 * 7680 + (16 + I) * 160 + j0 * 2);
      const float isc = *lds_at<float>(S_MISC + 16 + c2 * 4);
      const float dsk = bf_us(p.dskip[2 * pair + c2]);
#pragma unroll
      for (int k = 0; k < 4; ++k) {
        const float y0v = (k < 2 ? ya[2 * k] : yb[2 * k - 4]) * isc, y1v = (k < 2 ? ya[2 * k + 1] : yb[2 * k - 3]) * isc;
        const unsigned yy = pk_bf(y0v, y1v);
        const unsigned sk = pk_bf((float)uu[2 * k] * dsk, (float)uu[2 * k + 1] * dsk);
        res[c2][k] = pk_bf(bf_lo(yy) + bf_lo(sk), bf_hi(yy) + bf_hi(sk));
      }
    }
    u4 o0, o1;
#pragma unroll
    for (int k = 0; k < 2; ++k) {
      o0[2 * k] = __builtin_amdgcn_perm(res[1][k], res[0][k], 0x05040100u);
      o0[2 * k + 1] = __builtin_amdgcn_perm(res[1][k], res[0][k], 0x07060302u);
      o1[2 * k] = __builtin_amdgcn_perm(res[1][k + 2], res[0][k + 2], 0x05040100u);
      o1[2 * k + 1] = __builtin_amdgcn_perm(res[1][k + 2], res[0][k + 2], 0x07060302u);
    }
    const auto rs = __builtin_amdgcn_make_buffer_rsrc((void*)p.ws, 0, WS_END, 0x00020000);
    const unsigned dst = WS_AT + (pair * 2048 + 8 * tid) * 4;
    __builtin_amdgcn_raw_buffer_store_b128(o0, rs, dst, 0, 16);
    __builtin_amdgcn_raw_buffer_store_b128(o1, rs, dst + 16, 0, 16);
  }
  WAIT_VM(0);
  BAR_LDS();
  if (fused && tid == 0) st_flag(p.ws + WS_AFLAG + sb * 8, tag);
}

__device__ __forceinline__ void dma16(unsigned voff, const char* sbase, unsigned ldsaddr) {
  asm volatile("s_mov_b32 m0, %2\n\ts_nop 0\n\tglobal_load_lds_dwordx4 %0, %1" ::"v"(voff), "s"(sbase), "s"(ldsaddr) : "memory");
}
#define GLOAD(dst, voff, sbase, imm) \
  asm volatile("global_load_dwordx4 %0, %1, %2 offset:" #imm : "=v"(dst) : "v"(voff), "s"(sbase) : "memory")
template <int IMM>
__device__ __forceinline__ void gload(u4& dst, unsigned voff, const char* sbase) {
  asm volatile("global_load_dwordx4 %0, %1, %2 offset:%3" : "=v"(dst) : "v"(voff), "s"(sbase), "i"(IMM) : "memory");
}
template <int IMM>
__device__ __forceinline__ void gload_sc1(u4& dst, unsigned voff, const char* sbase) {
  asm volatile("global_load_dwordx4 %0, %1, %2 offset:%3 sc1" : "=v"(dst) : "v"(voff), "s"(sbase), "i"(IMM) : "memory");
}

constexpr unsigned G_SLOT1 = 24576;

template <int N> struct IC { static constexpr int value = N; };
template <int B, int E, typename Fn>
__device__ __forceinline__ void static_for(Fn&& fn) {
  if constexpr (B < E) { fn(IC<B>{}); static_for<B + 1, E>(fn); }
}

__device__ __forceinline__ float gelu_f(float x) {
  const float c0 = -2.f * 0.7978845608028654f * 1.4426950408889634f;
  const float c1 = c0 * 0.044715f;
  const float e = __builtin_amdgcn_exp2f(x * fmaf(x * x, c1, c0));
  return x * __builtin_amdgcn_rcpf(1.f + e);
}

__device__ __forceinline__ void g_gemm1(const Params& p, int gb, u64 tag, bool fused) {
  const int tid = threadIdx.x, w = __builtin_amdgcn_readfirstlane(tid >> 6), l = tid & 63;
  const int xg = gb & 7, j = gb >> 3;
  const int mt1 = 2 * xg + (j >> 4), ct = j & 15;
  const int sblk = ct < 11 ? 3 * ct : 32 + 2 * (ct - 11);
  const int f0 = 32 * sblk;
  const int tok = l & 15, g = l >> 4;
  const unsigned lb = lds_base();

  unsigned dsrc[6];
#pragma unroll
  for (int n = 0; n < 6; ++n) {
    const int st = 4 * n + g, ol = 4 * w + st / 6, fb = st % 6;
    const int qq = (l >> 1) & 7, k = 8 * ol + (qq ^ (4 * (ol & 1)));
    dsrc[n] = k * (FF * 2) + (f0 + 16 * fb + 8 * (l & 1)) * 2;
  }
  auto dma_chunk = [&](int c, int slot) {
    const char* sb = p.w1 + (size_t)c * 128 * (FF * 2);
#pragma unroll
    for (int n = 0; n < 6; ++n) dma16(dsrc[n], sb, lb + slot * G_SLOT1 + (24 * w + 4 * n) * 256);
  };
  u4 xf[16][2];
  const unsigned xo0 = (128 * mt1 + 32 * w + tok) * 1024 + g * 16, xo1 = xo0 + 16 * 1024;
  auto xload = [&](auto S) {
    constexpr int s = S.value;
    gload<s * 64>(xf[s][0], xo0, p.x);
    gload<s * 64>(xf[s][1], xo1, p.x);
  };
  dma_chunk(0, 0); static_for<0, 4>(xload);
  dma_chunk(1, 1); static_for<4, 8>(xload);
  dma_chunk(2, 2); static_for<8, 12>(xload);
  static_for<12, 16>(xload);

  f4 acc[6][2];
#pragma unroll
  for (int fb = 0; fb < 6; ++fb) { acc[fb][0] = f4{0.f, 0.f, 0.f, 0.f}; acc[fb][1] = f4{0.f, 0.f, 0.f, 0.f}; }
  const int q = tok >> 2, pp = l & 3;
  const unsigned a0 = g * 1536 + ((q ^ (4 * (g & 1))) * 32) + 8 * pp, a1 = a0 ^ 128;
  auto kstep = [&](auto S, int slot) {
    constexpr int s = S.value;
#pragma unroll
    for (int fb = 0; fb < 6; ++fb) {
      const unsigned off = slot * G_SLOT1 + (s & 3) * 6144 + fb * 256;
      const u2 lo = __builtin_bit_cast(u2, tr_read(a0 + off)), hi = __builtin_bit_cast(u2, tr_read(a1 + off));
      const b8 a = __builtin_bit_cast(b8, u4{lo[0], lo[1], hi[0], hi[1]});
      acc[fb][0] = __builtin_amdgcn_mfma_f32_16x16x32_bf16(a, __builtin_bit_cast(b8, xf[s][0]), acc[fb][0], 0, 0, 0);
      acc[fb][1] = __builtin_amdgcn_mfma_f32_16x16x32_bf16(a, __builtin_bit_cast(b8, xf[s][1]), acc[fb][1], 0, 0, 0);
    }
  };
#define XWAIT(n, c)                                                                                          \
  asm volatile("s_waitcnt vmcnt(" #n ")\n\ts_barrier"                                                        \
               : "+v"(xf[4 * c][0]), "+v"(xf[4 * c][1]), "+v"(xf[4 * c + 1][0]), "+v"(xf[4 * c + 1][1]),     \
                 "+v"(xf[4 * c + 2][0]), "+v"(xf[4 * c + 2][1]), "+v"(xf[4 * c + 3][0]), "+v"(xf[4 * c + 3][1]) \
               :: "memory")
  XWAIT(36, 0);
  static_for<0, 4>([&](auto S) { kstep(S, 0); });
  asm volatile("s_waitcnt lgkmcnt(0)" ::: "memory");
  XWAIT(22, 1);
  dma_chunk(3, 0);
  static_for<4, 8>([&](auto S) { kstep(S, 1); });
  XWAIT(14, 2);
  static_for<8, 12>([&](auto S) { kstep(S, 2); });
  XWAIT(0, 3);
  static_for<12, 16>([&](auto S) { kstep(S, 0); });
#undef XWAIT

  if (fused) {
    const bool act = l < 28;
    const char* fp = p.ws + WS_XNFLAG + (l < 16 ? 16 * mt1 + l : f0 / 8 + (l - 16)) * 8;
    poll_flags(fp, act, tag);
  }
  const auto rs = __builtin_amdgcn_make_buffer_rsrc((void*)p.ws, 0, WS_END, 0x00020000);
  float mu[2], rho[2];
#pragma unroll
  for (int tb = 0; tb < 2; ++tb) {
    const u2 st = __builtin_amdgcn_raw_buffer_load_b64(rs, WS_STATS + (128 * mt1 + 32 * w + 16 * tb + tok) * 8, 0, 16);
    rho[tb] = asf(st[1]); mu[tb] = -asf(st[0]) * rho[tb];
  }
#pragma unroll
  for (int sp = 0; sp < 3; ++sp) {
    unsigned hv[2][4];
#pragma unroll
    for (int hb = 0; hb < 2; ++hb) {
      const int fb = 2 * sp + hb;
      const u4 csv = __builtin_amdgcn_raw_buffer_load_b128(rs, WS_CS + (f0 + 16 * fb + 4 * g) * 4, 0, 16);
      const u2 bv = *(const u2*)(p.b1 + (f0 + 16 * fb + 4 * g) * 2);
      const float bb[4] = {bf_lo(bv[0]), bf_hi(bv[0]), bf_lo(bv[1]), bf_hi(bv[1])};
#pragma unroll
      for (int tb = 0; tb < 2; ++tb) {
        float o[4];
#pragma unroll
        for (int r = 0; r < 4; ++r) o[r] = gelu_f(fmaf(rho[tb], acc[fb][tb][r], fmaf(mu[tb], asf(csv[r]), bb[r])));
        hv[tb][2 * hb] = pk_bf(o[0], o[1]); hv[tb][2 * hb + 1] = pk_bf(o[2], o[3]);
      }
    }
    if (ct < 11 || sp > 0) {
#pragma unroll
      for (int tb = 0; tb < 2; ++tb)
        __builtin_amdgcn_raw_buffer_store_b128(u4{hv[tb][0], hv[tb][1], hv[tb][2], hv[tb][3]}, rs,
                                               WS_H + ((8 * mt1 + 2 * w + tb) * 43 + sblk + sp) * 1024 + l * 16, 0, 16);
    }
  }
  WAIT_VM(0);
  BAR_LDS();
  if (fused && tid == 0) st_flag(p.ws + WS_HFLAG + gb * 8, tag);
}

__device__ __forceinline__ void g_gemm2(const Params& p, int gb, u64 tag, bool fused) {
  const int tid = threadIdx.x, w = __builtin_amdgcn_readfirstlane(tid >> 6), l = tid & 63;
  const int xg = gb & 7, j = gb >> 3;
  const int mt2 = 4 * xg + (j >> 3), nt = j & 7;
  const int tok = l & 15, g = l >> 4;
  const unsigned lb = lds_base();
  const unsigned ring = w * 16384;

  const unsigned wsrc = (l >> 3) * 1024 + ((((l >> 1) & 3) ^ ((l >> 4) & 3)) * 32) + (l & 1) * 16 + nt * 128;
  auto dma_step = [&](int n) {
    int S = w + 4 * n; S = S < 43 ? S : 42;
    const char* sb = p.w2 + (size_t)S * 32768;
#pragma unroll
    for (int m = 0; m < 4; ++m) dma16(wsrc, sb + m * 8192, lb + ring + (n & 3) * 4096 + m * 1024);
  };
  dma_step(0); dma_step(1); dma_step(2); dma_step(3);

  if (fused) {
    const char* fp = p.ws + WS_HFLAG + (xg + 8 * (16 * (j >> 4) + (l & 15))) * 8;
    poll_flags(fp, l < 16, tag);
  }
  WAIT_VM(0);
  u4 hf[11][4];
  const char* hbase = p.ws + WS_H;
  const unsigned ho = (4 * mt2 * 43) * 1024 + l * 16;
  auto hload = [&](auto N) {
    constexpr int n = N.value;
    int S = w + 4 * n; S = S < 43 ? S : 42;
    const char* sb = hbase + (size_t)S * 1024;
    gload_sc1<0>(hf[n][0], ho, sb);
    gload_sc1<0>(hf[n][1], ho + 43 * 1024, sb);
    gload_sc1<0>(hf[n][2], ho + 2 * 43 * 1024, sb);
    gload_sc1<0>(hf[n][3], ho + 3 * 43 * 1024, sb);
  };
  static_for<0, 8>(hload);

  f4 acc[4][4];
#pragma unroll
  for (int cb = 0; cb < 4; ++cb)
#pragma unroll
    for (int tb = 0; tb < 4; ++tb) acc[cb][tb] = f4{0.f, 0.f, 0.f, 0.f};
  const int q = tok >> 2, pp = l & 3;
  const int sw = (2 * g + (q >> 1)) & 3;
  unsigned ab[4];
#pragma unroll
  for (int cb = 0; cb < 4; ++cb) ab[cb] = ring + (4 * g + q) * 128 + 8 * pp + ((cb ^ sw) * 32);

#define HWAIT(n, k) \
  asm volatile("s_waitcnt vmcnt(" #n ")" : "+v"(hf[k][0]), "+v"(hf[k][1]), "+v"(hf[k][2]), "+v"(hf[k][3])::"memory")
  auto step = [&](auto N) {
    constexpr int n = N.value;
    if constexpr (n == 0) HWAIT(28, 0);
    if constexpr (n == 1) HWAIT(32, 1);
    if constexpr (n == 2) HWAIT(36, 2);
    if constexpr (n == 3) HWAIT(40, 3);
    if constexpr (n == 4) HWAIT(24, 4);
    if constexpr (n == 5) HWAIT(20, 5);
    if constexpr (n == 6) HWAIT(16, 6);
    if constexpr (n == 7) HWAIT(12, 7);
    if constexpr (n == 8) HWAIT(8, 8);
    if constexpr (n == 9) HWAIT(4, 9);
    if constexpr (n == 10) HWAIT(0, 10);
    b8 a[4];
#pragma unroll
    for (int cb = 0; cb < 4; ++cb) {
      const u2 lo = __builtin_bit_cast(u2, tr_read(ab[cb] + (n & 3) * 4096));
      const u2 hi = __builtin_bit_cast(u2, tr_read(ab[cb] + (n & 3) * 4096 + 2048));
      a[cb] = __builtin_bit_cast(b8, u4{lo[0], lo[1], hi[0], hi[1]});
    }
    if constexpr (n + 4 <= 10) {
      asm volatile("s_waitcnt lgkmcnt(0)" : "+v"(a[0]), "+v"(a[1]), "+v"(a[2]), "+v"(a[3])::"memory");
      dma_step(n + 4);
    }
    if (n < 10 || w < 3) {
#pragma unroll
      for (int cb = 0; cb < 4; ++cb)
#pragma unroll
        for (int tb = 0; tb < 4; ++tb)
          acc[cb][tb] = __builtin_amdgcn_mfma_f32_16x16x32_bf16(a[cb], __builtin_bit_cast(b8, hf[n][tb]), acc[cb][tb], 0, 0, 0);
    }
    if constexpr (n + 8 <= 10) hload(IC<n + 8>{});
  };
  static_for<0, 11>(step);
#undef HWAIT

  BAR_LDS();
#pragma unroll
  for (int tb = 0; tb < 4; ++tb) {
    if (tb != w) {
      const int si = w < tb ? w : w - 1;
#pragma unroll
      for (int cb = 0; cb < 4; ++cb) *lds_at<f4>((tb * 3 + si) * 4096 + cb * 1024 + l * 16) = acc[cb][tb];
    }
  }
  f4 fin[4];
#pragma unroll
  for (int cb = 0; cb < 4; ++cb) fin[cb] = w == 0 ? acc[cb][0] : (w == 1 ? acc[cb][1] : (w == 2 ? acc[cb][2] : acc[cb][3]));
  if (fused) {
    const char* fp = p.ws + WS_AFLAG + (nt + 8 * (l & 31)) * 8;
    poll_flags(fp, l < 32, tag);
  }
  const int t = 64 * mt2 + 16 * w + tok;
  const auto rs = __builtin_amdgcn_make_buffer_rsrc((void*)p.ws, 0, WS_END, 0x00020000);
  u2 xv[4], bv[4];
  unsigned av[4][2];
#pragma unroll
  for (int cb = 0; cb < 4; ++cb) {
    const int ch = 64 * nt + 16 * cb + 4 * g;
    xv[cb] = *(const u2*)(p.x + (size_t)t * 1024 + ch * 2);
    bv[cb] = *(const u2*)(p.b2 + ch * 2);
    av[cb][0] = __builtin_amdgcn_raw_buffer_load_b32(rs, WS_AT + ((ch >> 1) * 2048 + t) * 4, 0, 16);
    av[cb][1] = __builtin_amdgcn_raw_buffer_load_b32(rs, WS_AT + (((ch >> 1) + 1) * 2048 + t) * 4, 0, 16);
  }
  BAR_LDS();
#pragma unroll
  for (int si = 0; si < 3; ++si)
#pragma unroll
    for (int cb = 0; cb < 4; ++cb) fin[cb] += *lds_at<f4>((w * 3 + si) * 4096 + cb * 1024 + l * 16);
#pragma unroll
  for (int cb = 0; cb < 4; ++cb) {
    const int ch = 64 * nt + 16 * cb + 4 * g;
    u2 o;
#pragma unroll
    for (int k = 0; k < 2; ++k) {
      const unsigned gm = pk_bf(fin[cb][2 * k], fin[cb][2 * k + 1]);
      const unsigned bo = pk_bf(bf_lo(gm) + bf_lo(bv[cb][k]), bf_hi(gm) + bf_hi(bv[cb][k]));
      const unsigned xa = pk_bf(bf_lo(xv[cb][k]) + bf_lo(av[cb][k]), bf_hi(xv[cb][k]) + bf_hi(av[cb][k]));
      o[k] = pk_bf(bf_lo(xa) + bf_lo(bo), bf_hi(xa) + bf_hi(bo));
    }
    *(u2*)(p.out + (size_t)t * 1024 + ch * 2) = o;
  }
}

__global__ void __launch_bounds__(256, 2) fused_kernel(Params p) {
  const int b = blockIdx.x;
  const bool fused = (p.mode & 16) != 0;
  u64 tag = 0;
  if (fused) tag = ld_flag(p.ws + WS_EPOCH) + 1;
  if (b < 256) {
    if (p.mode & 2) g_gemm1(p, b, tag, fused);
    if (p.mode & 8) { BAR_LDS(); g_gemm2(p, b, tag, fused); }
  } else {
    const int sb = b - 256;
    if (p.mode & 1) s_layernorm(p, sb, tag, fused);
    if (p.mode & 4) s_conv(p, sb, tag, fused);
    if (fused && sb == 0) {
      const int tid = threadIdx.x;
      poll_flags(p.ws + WS_XNFLAG + tid * 8, true, tag);
      poll_flags(p.ws + WS_HFLAG + tid * 8, true, tag);
      BAR_LDS();
      if (tid == 0) st_flag(p.ws + WS_EPOCH, tag);
    }
  }
}

}

extern "C" void kernel_launch(void* const* d_in, const int* in_sizes, int n_in, void* d_out, int out_size,
                              void* d_ws, size_t ws_size, hipStream_t stream) {
  (void)in_sizes; (void)n_in; (void)out_size; (void)ws_size;
  (void)hipFuncSetAttribute((const void*)fused_kernel, hipFuncAttributeMaxDynamicSharedMemorySize, LDS_BYTES);
  Params p;
  p.x = (const char*)d_in[0];
  p.logA = (const unsigned short*)d_in[1];
  p.Aim = (const unsigned short*)d_in[2];
  p.Cre = (const unsigned short*)d_in[3];
  p.Cim = (const unsigned short*)d_in[4];
  p.logdt = (const unsigned short*)d_in[5];
  p.dskip = (const unsigned short*)d_in[6];
  p.w1 = (const char*)d_in[8];
  p.b1 = (const char*)d_in[9];
  p.w2 = (const char*)d_in[10];
  p.b2 = (const char*)d_in[11];
  p.out = (char*)d_out;
  p.ws = (char*)d_ws;
  p.pad = 0;
  p.mode = 1;     fused_kernel<<<512, 256, LDS_BYTES, stream>>>(p);
  p.mode = 2 | 4; fused_kernel<<<512, 256, LDS_BYTES, stream>>>(p);
  p.mode = 8;     fused_kernel<<<512, 256, LDS_BYTES, stream>>>(p);
}
```

```cpp
#include <hip/hip_runtime.h>
#include <hip/hip_bf16.h>
#include <stdint.h>

#define SPLIT_LAUNCH 0

namespace {
#if defined(__HIP_DEVICE_COMPILE__)
#pragma clang attribute push(__attribute__((target("no-packed-fp32-ops"))), apply_to = function)
#endif

typedef float f2 __attribute__((ext_vector_type(2)));
typedef float f4 __attribute__((ext_vector_type(4)));
typedef float f16v __attribute__((ext_vector_type(16)));
typedef unsigned u2 __attribute__((ext_vector_type(2)));
typedef unsigned u4 __attribute__((ext_vector_type(4)));
typedef short s4 __attribute__((ext_vector_type(4)));
typedef _Float16 h2 __attribute__((ext_vector_type(2)));
typedef __bf16 b2 __attribute__((ext_vector_type(2)));
typedef unsigned long long u64;
#define LAS __attribute__((address_space(3)))
#define GAS __attribute__((address_space(1)))
typedef GAS u64 gu64;

constexpr int SEQ = 2048, DM = 512, NST = 128, FF = 1376;

#define HAS_STRIDES 1
#define H32 1
constexpr unsigned WS_EPOCH = 0;
constexpr unsigned WS_XNFLAG = 4096;
constexpr unsigned WS_HFLAG = 4096 + 2048;
constexpr unsigned WS_AFLAG = 4096 + 4096;
constexpr unsigned WS_STATS = 16384;
constexpr unsigned XNB_STRIDE = 8192 + 128, AT_STRIDE = 8192 + 128;
constexpr unsigned WS_XNB = 65536;
constexpr unsigned WS_AT = WS_XNB + 256 * XNB_STRIDE;
constexpr unsigned WS_H = WS_AT + 256 * AT_STRIDE;
constexpr unsigned WS_TRASH = WS_H + 64 * 86 * 1024;
constexpr unsigned WS_END = WS_TRASH + 256 * 8192;

constexpr int LDS_BYTES = 81920;

struct Params {
  const char* x;
  const unsigned short* logA;
  const unsigned short* Aim;
  const unsigned short* Cre;
  const unsigned short* Cim;
  const unsigned short* logdt;
  const unsigned short* dskip;
  const char* w1;
  const char* b1;
  const char* w2;
  const char* b2;
  char* out;
  char* ws;
};

__device__ __forceinline__ float bf_lo(unsigned u) { return __builtin_bit_cast(float, u << 16); }
__device__ __forceinline__ float bf_hi(unsigned u) { return __builtin_bit_cast(float, u & 0xffff0000u); }
__device__ __forceinline__ float bf_us(unsigned short u) { return __builtin_bit_cast(float, (unsigned)u << 16); }
__device__ __forceinline__ unsigned pk_bf(float a, float b) { b2 v = {(__bf16)a, (__bf16)b}; return __builtin_bit_cast(unsigned, v); }
__device__ __forceinline__ unsigned pk_h(float a, float b) { h2 v = {(_Float16)a, (_Float16)b}; return __builtin_bit_cast(unsigned, v); }
__device__ __forceinline__ float bfr(float a) { return bf_lo(pk_bf(a, a)); }
__device__ __forceinline__ float asf(unsigned u) { return __builtin_bit_cast(float, u); }
__device__ __forceinline__ unsigned asu(float u) { return __builtin_bit_cast(unsigned, u); }
__device__ __forceinline__ float h_lo(unsigned u) { return (float)__builtin_bit_cast(h2, u)[0]; }
__device__ __forceinline__ float h_hi(unsigned u) { return (float)__builtin_bit_cast(h2, u)[1]; }

template <int CTRL>
__device__ __forceinline__ float dpp_add(float x) {
  return x + asf((unsigned)__builtin_amdgcn_update_dpp(0, (int)asu(x), CTRL, 0xf, 0xf, true));
}
__device__ __forceinline__ float sum16(float x) {
  x = dpp_add<0x128>(x); x = dpp_add<0x124>(x); x = dpp_add<0x122>(x); x = dpp_add<0x121>(x);
  return x;
}
__device__ __forceinline__ float swap16_add(float a, float b) {
  auto r = __builtin_amdgcn_permlane16_swap(asu(a), asu(b), false, false);
  return asf(r[0]) + asf(r[1]);
}
__device__ __forceinline__ float swap32_add(float a, float b) {
  auto r = __builtin_amdgcn_permlane32_swap(asu(a), asu(b), false, false);
  return asf(r[0]) + asf(r[1]);
}
__device__ __forceinline__ float sum32(float x) { x = sum16(x); return swap16_add(x, x); }
__device__ __forceinline__ float sum64(float x) { x = sum32(x); return swap32_add(x, x); }

struct cplx { float x, y, ny; };
__device__ __forceinline__ f2 cmul(const cplx& a, f2 b) {
  return f2{__builtin_fmaf(a.ny, b.y, a.x * b.x), __builtin_fmaf(a.y, b.x, a.x * b.y)};
}
__device__ __forceinline__ cplx cx(f2 v) { return cplx{v.x, v.y, -v.y}; }

#define STAMP_INSN "s_memrealtime"
#define STAMP(i)
#define STAMP_ARG
#define STAMP_PASS

#define BAR_LDS() asm volatile("s_waitcnt lgkmcnt(0)\n\ts_barrier" ::: "memory")
#define WAIT_VM(n) asm volatile("s_waitcnt vmcnt(" #n ")" ::: "memory")
#define SCHED_FENCE() __builtin_amdgcn_sched_barrier(0)
template <int N> __device__ __forceinline__ void wait_vm() { asm volatile("s_waitcnt vmcnt(%0)" ::"i"(N) : "memory"); }
template <int N> __device__ __forceinline__ void wait_lgkm() { asm volatile("" ::: "memory"); __builtin_amdgcn_s_waitcnt(0xC07F | (N << 8)); asm volatile("" ::: "memory"); }

template <int N> struct IC { static constexpr int value = N; };
template <int B, int E, typename Fn>
__device__ __forceinline__ void static_for(Fn&& fn) {
  if constexpr (B < E) { fn(IC<B>{}); static_for<B + 1, E>(fn); }
}

__device__ __forceinline__ bool all_lanes(bool c) { return __builtin_amdgcn_ballot_w64(!c) == 0; }
__device__ __forceinline__ float exp_acc(float x) {
  const float L = 1.44269502162933349609375f, Llo = 1.925963033500011e-8f;
  const float t = x * L, r = __builtin_fmaf(x, Llo, __builtin_fmaf(x, L, -t));
  const float e = __builtin_amdgcn_exp2f(t);
  return __builtin_fmaf(e, r * 0.6931471805599453f, e);
}
__device__ __forceinline__ u64 ld_flag(const char* p) {
  return __hip_atomic_load((gu64*)p, __ATOMIC_RELAXED, __HIP_MEMORY_SCOPE_AGENT);
}
__device__ __forceinline__ void st_flag(char* p, u64 v) {
  __hip_atomic_store((gu64*)p, v, __ATOMIC_RELAXED, __HIP_MEMORY_SCOPE_AGENT);
}
#define POLL_SLEEP 4
__device__ __forceinline__ void poll_flags(const char* p, bool active, u64 tag) {
  for (int spins = 0; spins < (1 << 20); ++spins) {
    u64 v = tag;
    if (active) v = ld_flag(p);
    if (all_lanes(v == tag)) break;
    __builtin_amdgcn_s_sleep(POLL_SLEEP);
  }
}

template <typename T>
__device__ __forceinline__ LAS T* lds_at(unsigned off) {
  extern __shared__ __attribute__((aligned(16))) char smem[];
  return (LAS T*)((LAS char*)smem + off);
}
__device__ __forceinline__ unsigned lds_base() {
  extern __shared__ __attribute__((aligned(16))) char smem[];
  return (unsigned)(size_t)(LAS char*)smem;
}
__device__ __forceinline__ u2 tr_read(unsigned off) {
  return __builtin_bit_cast(u2, __builtin_amdgcn_ds_read_tr16_b64_v4i16(lds_at<s4>(off)));
}
template <typename T> __device__ __forceinline__ T sel(bool c, T a, T b) { return c ? a : b; }
__device__ __forceinline__ u4 join(u2 lo, u2 hi) { return u4{lo[0], lo[1], hi[0], hi[1]}; }

__device__ __forceinline__ u4 make_srd(const void* p, unsigned bytes) {
  const u64 a = (u64)p;
  return u4{(unsigned)a, (unsigned)(a >> 32) & 0xffffu, bytes, 0x00020000u};
}
__device__ __forceinline__ void set_m0(unsigned v) { asm volatile("s_mov_b32 m0, %0" ::"s"(v) : "memory"); }
template <int IMM>
__device__ __forceinline__ void dma(unsigned voff, const u4& srd, unsigned soff) {
  asm volatile("buffer_load_dwordx4 %0, %1, %2 offen offset:%3 lds" ::"v"(voff), "s"(srd), "s"(soff), "i"(IMM) : "memory");
}
__device__ __forceinline__ void dma_sc1(unsigned voff, const u4& srd, unsigned soff) {
  asm volatile("buffer_load_dwordx4 %0, %1, %2 offen sc1 lds" ::"v"(voff), "s"(srd), "s"(soff) : "memory");
}
template <int IMM>
__device__ __forceinline__ void dma_nop(unsigned voff, const u4& srd, unsigned soff) {
  asm volatile("s_nop 0\n\tbuffer_load_dwordx4 %0, %1, %2 offen offset:%3 lds" ::"v"(voff), "s"(srd), "s"(soff), "i"(IMM) : "memory");
}
template <int IMM>
__device__ __forceinline__ void bld4(u4& d, unsigned voff, const u4& srd, unsigned soff) {
  asm volatile("buffer_load_dwordx4 %0, %1, %2, %3 offen offset:%4" : "=v"(d) : "v"(voff), "s"(srd), "s"(soff), "i"(IMM) : "memory");
}
template <int IMM>
__device__ __forceinline__ void bld4_sc1(u4& d, unsigned voff, const u4& srd, unsigned soff) {
  asm volatile("buffer_load_dwordx4 %0, %1, %2, %3 offen offset:%4 sc1" : "=v"(d) : "v"(voff), "s"(srd), "s"(soff), "i"(IMM) : "memory");
}
template <int IMM>
__device__ __forceinline__ void bld2(u2& d, unsigned voff, const u4& srd, unsigned soff) {
  asm volatile("buffer_load_dwordx2 %0, %1, %2, %3 offen offset:%4" : "=v"(d) : "v"(voff), "s"(srd), "s"(soff), "i"(IMM) : "memory");
}
template <int IMM>
__device__ __forceinline__ void bld2_sc1(u2& d, unsigned voff, const u4& srd, unsigned soff) {
  asm volatile("buffer_load_dwordx2 %0, %1, %2, %3 offen offset:%4 sc1" : "=v"(d) : "v"(voff), "s"(srd), "s"(soff), "i"(IMM) : "memory");
}
__device__ __forceinline__ void bld1_sc1(unsigned& d, unsigned voff, const u4& srd, unsigned soff) {
  asm volatile("buffer_load_dword %0, %1, %2, %3 offen sc1" : "=v"(d) : "v"(voff), "s"(srd), "s"(soff) : "memory");
}
__device__ __forceinline__ void st_flag_asm(const u4& srd, unsigned off, u64 v) {
  const u2 d = {(unsigned)v, (unsigned)(v >> 32)};
  asm volatile("buffer_store_dwordx2 %0, %1, %2, 0 offen sc1" ::"v"(d), "v"(off), "s"(srd) : "memory");
}
__device__ __forceinline__ void touch(const char* base, unsigned off, unsigned lds_sink) {
  asm volatile("s_mov_b32 m0, %2\n\ts_nop 0\n\tglobal_load_lds_dword %0, %1" ::"v"(off), "s"(base), "s"(lds_sink) : "memory");
}

__device__ __forceinline__ void mma32(f16v& c, const u4& a, const u4& b) {
  asm volatile("v_mfma_f32_32x32x16_bf16 %0, %1, %2, %0" : "+v"(c) : "v"(a), "v"(b) : "memory");
}
__device__ __forceinline__ void mma16(f4& c, const u4& a, const u4& b) {
  asm volatile("v_mfma_f32_16x16x32_bf16 %0, %1, %2, %0" : "+v"(c) : "v"(a), "v"(b) : "memory");
}
__device__ __forceinline__ void mma16h(f4& c, const u4& a, const u4& b) {
  asm volatile("v_mfma_f32_16x16x32_f16 %0, %1, %2, %0" : "+v"(c) : "v"(a), "v"(b) : "memory");
}
#define MFMA_DRAIN() asm volatile("s_nop 7\n\ts_nop 7" ::: "memory")

constexpr unsigned S_TB = 0;
constexpr unsigned S_TA = 43520;
constexpr unsigned S_R = 70720;
constexpr unsigned S_STAGE = 70720;
constexpr unsigned S_MISC = 79424;
constexpr unsigned S_SINK = 79488;
constexpr unsigned S_DUMMY = 79744;
constexpr unsigned S_CP = 0;
constexpr unsigned S_U = 35328;
constexpr unsigned S_EX = 0;
constexpr unsigned S_AO = 16384;

__device__ __forceinline__ constexpr int row_of(int e) {
  return e < 16 ? e : (e < 32 ? 16 + (e - 16) / 2 : (e < 64 ? 24 + (e - 32) / 4 : 32 + (e - 64) / 8));
}

template <bool fused, bool do_ln, bool do_conv>
__device__ __forceinline__ void s_role(const Params& p, int sb, u64& tag STAMP_ARG) {
  const int tid = threadIdx.x, w = __builtin_amdgcn_readfirstlane(tid >> 6), l = tid & 63;
  const int chn = w >> 1, hf = w & 1;
  const int pair = 32 * (sb & 7) + (sb >> 3);
  const int ch = 2 * pair + chn;
  const int j16 = l & 15, g = l >> 4;
  const auto rs = __builtin_amdgcn_make_buffer_rsrc((void*)p.ws, 0, WS_END, 0x00020000);

  u4 xa = {0, 0, 0, 0}, xb = {0, 0, 0, 0};
  const int blk = 32 * (sb & 7) + (sb >> 3);
  const int c = l & 31, tl = 2 * w + (l >> 5), t = 8 * blk + tl;
  if constexpr (do_ln) {
    const u4* xr = (const u4*)(p.x + (size_t)t * 1024);
    xa = xr[c]; xb = xr[32 + c];
  }
  if constexpr (fused) tag = ld_flag(p.ws + WS_EPOCH) + 1;
  unsigned short r_dt = 0, r_la = 0, r_ai = 0, r_cr = 0, r_ci = 0, r_d = 0;
  const int pi = ch * NST + 64 * hf + l;
  if constexpr (do_conv) {
    r_dt = p.logdt[ch]; r_la = p.logA[pi]; r_ai = p.Aim[pi]; r_cr = p.Cre[pi]; r_ci = p.Cim[pi];
    r_d = p.dskip[ch];
  }

  if constexpr (do_ln) {
    float v[16];
#pragma unroll
    for (int i = 0; i < 4; ++i) {
      v[2 * i] = bf_lo(xa[i]); v[2 * i + 1] = bf_hi(xa[i]);
      v[8 + 2 * i] = bf_lo(xb[i]); v[8 + 2 * i + 1] = bf_hi(xb[i]);
    }
    STAMP(1);
    float s0 = v[0] + v[8], s1 = v[1] + v[9];
#pragma unroll
    for (int i = 2; i < 8; i += 2) { s0 += v[i]; s1 += v[i + 1]; s0 += v[8 + i]; s1 += v[9 + i]; }
    const float mean = sum32(s0 + s1) * (1.f / 512.f);
    float q0 = 0.f, q1 = 0.f;
#pragma unroll
    for (int i = 0; i < 16; i += 2) { const float d0 = v[i] - mean, d1 = v[i + 1] - mean; q0 = __builtin_fmaf(d0, d0, q0); q1 = __builtin_fmaf(d1, d1, q1); }
    const float var = sum32(q0 + q1) * (1.f / 512.f);
    const float m = bfr(mean);
    const float sd = bfr(__builtin_amdgcn_sqrtf(bfr(bfr(var) + 1e-5f)));
    const float r1 = __builtin_amdgcn_rcpf(sd), mr = -m * r1;
    unsigned hx[8];
#pragma unroll
    for (int i = 0; i < 8; ++i) hx[i] = pk_h(__builtin_fmaf(v[2 * i], r1, mr), __builtin_fmaf(v[2 * i + 1], r1, mr));
    *lds_at<u4>(S_STAGE + tl * 1088 + c * 16) = u4{hx[0], hx[1], hx[2], hx[3]};
    *lds_at<u4>(S_STAGE + tl * 1088 + 512 + c * 16) = u4{hx[4], hx[5], hx[6], hx[7]};
    const float m2 = bfr((mean - m) * r1);
    const float sd2 = bfr(__builtin_amdgcn_sqrtf(bfr(bfr(var * r1 * r1) + 1e-5f)));
    const float mu = __builtin_fmaf(m2, sd, m);
    const float rho = __builtin_amdgcn_rcpf(sd * sd2);
    if (c == 0)
      __builtin_amdgcn_raw_buffer_store_b128(u4{asu(mu), (unsigned)tag, asu(rho), (unsigned)(tag >> 32)}, rs, WS_STATS + t * 16, 0, 16);
    BAR_LDS();
#pragma unroll
    for (int ps = 0; ps < 2; ++ps) {
      const int cb = 128 * w + 64 * ps + 16 * g;
      const int q = j16 >> 2, pp = l & 3;
      const u2 lo = tr_read(S_STAGE + q * 1088 + (cb + 4 * pp) * 2);
      const u2 hi = tr_read(S_STAGE + (4 + q) * 1088 + (cb + 4 * pp) * 2);
      __builtin_amdgcn_raw_buffer_store_b128(join(lo, hi), rs, WS_XNB + blk * XNB_STRIDE + (128 * w + 64 * ps + l) * 16, 0, 16);
    }
    STAMP(2);
    if constexpr (!do_conv) { WAIT_VM(0); BAR_LDS(); if (fused && tid == 0) st_flag(p.ws + WS_XNFLAG + blk * 8, tag); }
  }
  if constexpr (do_conv) {
  const float dt = bfr(exp_acc(bf_us(r_dt)));
  const float ar = -bfr(exp_acc(bf_us(r_la)));
  const float zr = ar * dt, zi = bf_us(r_ai) * dt;
  const float cr = bf_us(r_cr) * dt, ci = bf_us(r_ci) * dt;
  const float bound = sum64(__builtin_fabsf(cr) + __builtin_fabsf(ci));
  const int eb = __builtin_amdgcn_frexp_expf(bound);
  const float sA = __builtin_amdgcn_ldexpf(1.f, 8 - eb), isA = __builtin_amdgcn_ldexpf(1.f, eb - 8);
  const f2 C = {cr * sA, ci * sA};
  const float a2 = zr * 1.44269504088896341f, b2r = zi * 0.15915494309189535f;
  auto direct = [&](float e) -> f2 {
    const float mag = __builtin_amdgcn_exp2f(a2 * e);
    const float fr = __builtin_amdgcn_fractf(b2r * e);
    return f2{mag * __builtin_amdgcn_cosf(fr), mag * __builtin_amdgcn_sinf(fr)};
  };
  const unsigned tb = S_TB + w * (40 * 272) + l * 4, ta = S_TA + w * (25 * 272) + l * 4;
  auto putB = [&](int row, f2 v) { *lds_at<unsigned>(tb + row * 272) = pk_h(v.x, v.y); };
  auto putA = [&](int row, f2 v) { *lds_at<unsigned>(ta + row * 272) = pk_h(v.x, -v.y); };

  f2 P[40];
  P[0] = f2{1.f, 0.f};
  P[1] = direct(1.f); P[2] = direct(2.f); P[4] = direct(4.f); P[8] = direct(8.f);
  P[row_of(16)] = direct(16.f); P[row_of(32)] = direct(32.f); P[row_of(64)] = direct(64.f);
  const f2 P128 = direct(128.f), P256 = direct(256.f), P512 = direct(512.f), P1024 = direct(1024.f);
  STAMP(3);
  {
    const cplx c2 = cx(P[2]), c4 = cx(P[4]), c8 = cx(P[8]);
    P[3] = cmul(c2, P[1]); P[5] = cmul(c4, P[1]); P[6] = cmul(c4, P[2]); P[7] = cmul(c4, P[3]);
#pragma unroll
    for (int e = 9; e < 16; ++e) P[e] = cmul(c8, P[e - 8]);
    const cplx c16 = cx(P[row_of(16)]), c32 = cx(P[row_of(32)]), c64 = cx(P[row_of(64)]);
#pragma unroll
    for (int e = 18; e < 32; e += 2) P[row_of(e)] = cmul(c16, P[row_of(e - 16)]);
#pragma unroll
    for (int e = 36; e < 64; e += 4) P[row_of(e)] = cmul(c32, P[row_of(e - 32)]);
#pragma unroll
    for (int e = 72; e < 128; e += 8) P[row_of(e)] = cmul(c64, P[row_of(e - 64)]);
  }
#pragma unroll
  for (int r = 0; r < 40; ++r) putB(r, P[r]);
  if constexpr (do_ln) {
    WAIT_VM(0);
    asm volatile("s_barrier" ::: "memory");
    if (fused && tid == 0) st_flag(p.ws + WS_XNFLAG + blk * 8, tag);
  }
  const cplx cC = cx(C);
  putA(0, C);
#pragma unroll
  for (int i = 1; i < 8; ++i) putA(i, cmul(cC, P[row_of(16 * i)]));
  {
    const f2 Q128 = cmul(cC, P128);
    const cplx q = cx(Q128);
    putA(8, Q128);
#pragma unroll
    for (int i = 1; i < 8; ++i) putA(8 + i, cmul(q, P[row_of(16 * i)]));
  }

  const unsigned rb = S_TB + w * (40 * 272) + g * 16, ra = S_TA + w * (25 * 272) + g * 16;
  auto seg_mfma = [&](unsigned arow, unsigned brow) -> f4 {
    typedef _Float16 h8 __attribute__((ext_vector_type(8)));
    f4 acc = {0.f, 0.f, 0.f, 0.f};
#pragma unroll
    for (int s = 0; s < 4; ++s) {
      const h8 a = *lds_at<h8>(ra + arow * 272 + s * 64);
      const h8 b = *lds_at<h8>(rb + brow * 272 + s * 64);
      acc = __builtin_amdgcn_mfma_f32_16x16x32_f16(a, b, acc, 0, 0, 0);
    }
    return acc;
  };
  auto row_rt = [&](int e) -> unsigned {
    return e < 16 ? e : (e < 32 ? 16 + ((e - 16) >> 1) : (e < 64 ? 24 + ((e - 32) >> 2) : 32 + ((e - 64) >> 3)));
  };
  f4 D1 = seg_mfma(j16, j16);
  {
    const f2 P32 = P[row_of(32)], P64 = P[row_of(64)], P96 = P[row_of(96)];
    const cplx c128 = cx(P128), c256 = cx(P256), c512 = cx(P512);
    const f2 P160 = cmul(c128, P32), P192 = cmul(c128, P64), P224 = cmul(c128, P96);
    const f2 Q256 = cmul(cC, P256);
    const cplx q2 = cx(Q256);
    putA(0, Q256); putA(1, cmul(q2, P32)); putA(2, cmul(q2, P64)); putA(3, cmul(q2, P96));
    putA(4, cmul(q2, P128)); putA(5, cmul(q2, P160)); putA(6, cmul(q2, P192)); putA(7, cmul(q2, P224));
    const f2 P320 = cmul(c256, P64), P384 = cmul(c256, P128), P448 = cmul(c256, P192);
    const f2 Q512 = cmul(cC, P512);
    const cplx q4 = cx(Q512);
    putA(8, Q512); putA(9, cmul(q4, P64)); putA(10, cmul(q4, P128)); putA(11, cmul(q4, P192));
    putA(12, cmul(q4, P256)); putA(13, cmul(q4, P320)); putA(14, cmul(q4, P384)); putA(15, cmul(q4, P448));
    const f2 P640 = cmul(c512, P128), P768 = cmul(c512, P256), P896 = cmul(c512, P384);
    const f2 Q1024 = cmul(cC, P1024);
    const cplx q8 = cx(Q1024);
    putA(16, Q1024); putA(17, cmul(q8, P128)); putA(18, cmul(q8, P256)); putA(19, cmul(q8, P384));
    putA(20, cmul(q8, P512)); putA(21, cmul(q8, P640)); putA(22, cmul(q8, P768)); putA(23, cmul(q8, P896));
    putA(24, cmul(q8, P1024));
  }
  const char* xfp = p.ws + WS_XNFLAG + (32 * w + (l & 31) + 128 * (l >> 5)) * 8;
  u64 xfl = tag;
  if constexpr (fused) xfl = ld_flag(xfp);
  const int i8 = j16 < 8 ? j16 : 7;
  f4 D2 = seg_mfma(i8, row_rt(2 * j16));
  f4 D4 = seg_mfma(8 + i8, row_rt(4 * j16));
  f4 D8 = seg_mfma(16 + (j16 < 9 ? j16 : 8), row_rt(8 * j16));
  STAMP(4);
  {
    const unsigned ex = S_TA + w * (25 * 272);
    *lds_at<f4>(ex + l * 16) = sel<f4>(hf, D8, D2) * isA;
    *lds_at<f4>(ex + 1024 + l * 16) = sel<f4>(hf, D4, D1) * isA;
    if (l == 0) *lds_at<float>(S_MISC + w * 4) = bound;
  }
  u4 ua = {0, 0, 0, 0}, ub = {0, 0, 0, 0};
  const unsigned usrc = WS_XNB + (tid >> 1) * XNB_STRIDE + pair * 32 + (tid & 1) * 16;
  const bool early = all_lanes(xfl == tag);
  if (early) {
    ua = __builtin_amdgcn_raw_buffer_load_b128(rs, usrc, 0, 16);
    ub = __builtin_amdgcn_raw_buffer_load_b128(rs, usrc + 128 * XNB_STRIDE, 0, 16);
  }
  if (tid < 2 * 64) {
    const int c2 = tid >> 6, e = tid & 63;
    *lds_at<unsigned short>(S_R + c2 * 4224 + (e < 9 ? e : 2048 + e) * 2) = 0;
  }
  BAR_LDS();
  {
    const unsigned ex = S_TA + (w ^ 1) * (25 * 272);
    const f4 o0 = *lds_at<f4>(ex + l * 16), o1 = *lds_at<f4>(ex + 1024 + l * 16);
    const float btot = 2.f * (bound + *lds_at<float>(S_MISC + (w ^ 1) * 4));
    const int et = __builtin_amdgcn_frexp_expf(btot);
    const float sc = __builtin_amdgcn_ldexpf(2.f, 15 - et);
    const unsigned rb0 = S_R + chn * 4224;
    const bool ev = !(j16 & 1), ok = g < 2;
    auto st32 = [&](unsigned a, unsigned v) { *lds_at<unsigned>(a) = v; };
    auto st16 = [&](unsigned a, unsigned v) { *lds_at<unsigned short>(a) = (unsigned short)v; };
    if (hf == 0) {
      unsigned k8[4], k4[4];
#pragma unroll
      for (int r = 0; r < 4; ++r) {
        const float a = __builtin_fmaf(D8[r], isA, o0[r]) * sc, b = __builtin_fmaf(D4[r], isA, o1[r]) * sc;
        k8[r] = pk_h(a, a); k4[r] = pk_h(b, b);
      }
      {
        const int p3 = 1024 + 128 * (4 * g + 3) + 8 * j16;
        const unsigned a32 = ok ? rb0 + 2 * ((ev ? 2052 : 2054) - p3) : S_DUMMY;
        const unsigned a32e = ev ? a32 : S_DUMMY;
        const unsigned a16 = ok ? rb0 + 2 * ((ev ? 2060 : 2053) - p3) : S_DUMMY;
#pragma unroll
        for (int r = 0; r < 4; ++r) {
          const unsigned o = (3 - r) * 256;
          st32(a32 + o, k8[r]); st32(a32 + o + 4, k8[r]); st32(a32 + o + 8, k8[r]); st32(a32e + o + 12, k8[r]);
          st16(a16 + o, k8[r]);
        }
        if (l == 32) { st16(rb0 + 18, k8[0]); st32(rb0 + 20, k8[0]); st16(rb0 + 24, k8[0]); }
      }
      {
        const int p3 = 512 + 64 * (4 * g + 3) + 4 * j16;
        const unsigned a32 = ok ? rb0 + 2 * ((ev ? 2054 : 2056) - p3) : S_DUMMY;
        const unsigned a32e = ev ? a32 : S_DUMMY;
        const unsigned a16 = ok ? rb0 + 2 * ((ev ? 2058 : 2055) - p3) : S_DUMMY;
#pragma unroll
        for (int r = 0; r < 4; ++r) {
          const unsigned o = (3 - r) * 128;
          st32(a32 + o, k4[r]); st32(a32e + o + 4, k4[r]);
          st16((r == 0 && l == 0 ? S_DUMMY : a16) + o, k4[r]);
        }
      }
    } else {
      unsigned k2[4], k1[4];
#pragma unroll
      for (int r = 0; r < 4; ++r) {
        const float a = __builtin_fmaf(D2[r], isA, o0[r]) * sc, b = __builtin_fmaf(D1[r], isA, o1[r]) * sc;
        k2[r] = pk_h(a, a); k1[r] = pk_h(b, b);
      }
      const int p3 = 256 + 32 * (4 * g + 3) + 2 * j16;
      const unsigned a32e = ok && ev ? rb0 + 2 * (2056 - p3) : S_DUMMY;
      const unsigned a16 = ok ? rb0 + 2 * ((ev ? 2055 : 2056) - p3) : S_DUMMY;
#pragma unroll
      for (int r = 0; r < 4; ++r) { st32(a32e + (3 - r) * 64, k2[r]); st16(a16 + (3 - r) * 64, k2[r]); }
      const unsigned a1 = rb0 + 2 * (2056 - (16 * (4 * g + 3) + j16));
#pragma unroll
      for (int r = 0; r < 4; ++r) st16(a1 + (3 - r) * 32, k1[r]);
    }
    if (l == 0 && hf == 0) *lds_at<float>(S_MISC + 16 + chn * 4) = __builtin_amdgcn_ldexpf(1.f, et - 15);
  }
  BAR_LDS();
  STAMP(5);
#pragma unroll
  for (int it = 0; it < 3; ++it) {
    int c2, Y;
    if (it < 2) { c2 = it; Y = tid; } else { c2 = tid >> 3; Y = 256 + (tid & 7); }
    if (it < 2 || (tid < 16 && (tid & 7) < 5)) {
      const u4 lo = *lds_at<u4>(S_R + c2 * 4224 + Y * 16), hi = *lds_at<u4>(S_R + c2 * 4224 + Y * 16 + 16);
      const unsigned o2 = __builtin_amdgcn_alignbit(lo[3], lo[2], 16), o3 = __builtin_amdgcn_alignbit(hi[0], lo[3], 16);
      const unsigned o4 = __builtin_amdgcn_alignbit(hi[1], hi[0], 16), o5 = __builtin_amdgcn_alignbit(hi[2], hi[1], 16);
      const unsigned o6 = __builtin_amdgcn_alignbit(hi[3], hi[2], 16);
      const unsigned dst = S_CP + c2 * 17664 + Y * 16;
      *lds_at<u4>(dst) = hi;
      *lds_at<u4>(dst + 4416) = u4{o3, o4, o5, o6};
      *lds_at<u4>(dst + 2 * 4416) = u4{lo[3], hi[0], hi[1], hi[2]};
      *lds_at<u4>(dst + 3 * 4416) = u4{o2, o3, o4, o5};
    }
  }
  if (tid < 160) { *lds_at<u4>(S_U + tid * 16) = u4{0, 0, 0, 0}; *lds_at<u4>(S_U + 7680 + tid * 16) = u4{0, 0, 0, 0}; }
  STAMP(6);
  if (!early) {
    poll_flags(xfp, true, tag);
    ua = __builtin_amdgcn_raw_buffer_load_b128(rs, usrc, 0, 16);
    ub = __builtin_amdgcn_raw_buffer_load_b128(rs, usrc + 128 * XNB_STRIDE, 0, 16);
  }
  {
    const int bk = tid >> 1;
    const unsigned dst = S_U + (tid & 1) * 7680 + (16 + (bk >> 3)) * 160 + (bk & 7) * 16;
    *lds_at<u4>(dst) = ua;
    *lds_at<u4>(dst + 16 * 160) = ub;
  }
  BAR_LDS();
  STAMP(7);
  f4 acc[2][4];
  {
    const int a = j16 >> 2, b = j16 & 3;
    const unsigned abase = S_CP + chn * 17664 + b * 4416 + (2048 - 16 * 127 - 4 * a + 8 * g) * 2;
    const unsigned ubase = S_U + chn * 7680 + j16 * 160 + g * 16;
    auto afrag = [&](int ww) -> u4 {
      const u2 lo = *lds_at<volatile u2>(abase + (127 - ww) * 32), hi = *lds_at<volatile u2>(abase + (127 - ww) * 32 + 8);
      return join(lo, hi);
    };
    auto bfrag = [&](int ct, int q) -> u4 {
      const int off = (16 + 16 * ct) * 160 + ((q & 1) ? -160 * ((q - 1) / 2) : -160 * (q / 2) + 64);
      return *lds_at<u4>(ubase + off);
    };
    u4 A[4][2], B[3][2];
    auto run = [&](auto Q0, auto Q1) {
      constexpr int q0 = Q0.value, q1 = Q1.value;
#pragma unroll
      for (int ct = 0; ct < 2; ++ct)
#pragma unroll
        for (int r = 0; r < 4; ++r) { acc[ct][r] = f4{0.f, 0.f, 0.f, 0.f}; asm volatile("" : "+v"(acc[ct][r])); }
      auto fetch_a = [&](auto Q) { constexpr int q = Q.value; A[q & 3][0] = afrag(2 * q); A[q & 3][1] = afrag(2 * q + 1); };
      auto fetch_b = [&](auto Q, auto CT) { constexpr int q = Q.value, ct = CT.value; if constexpr (ct == 1 || q < 32) B[q % 3][ct] = bfrag(ct, q); };
      if constexpr (q0 > 0) fetch_a(IC<q0 - 1>{});
      fetch_a(IC<q0>{}); fetch_b(IC<q0>{}, IC<0>{}); fetch_b(IC<q0>{}, IC<1>{});
      SCHED_FENCE();
      fetch_a(IC<q0 + 1>{}); fetch_b(IC<q0 + 1>{}, IC<0>{}); fetch_b(IC<q0 + 1>{}, IC<1>{});
      SCHED_FENCE();
      asm volatile("s_nop 1" ::: "memory");
      static_for<q0, q1>([&](auto Q) {
        constexpr int q = Q.value;
        constexpr bool more = q + 2 < q1, prev_more = q + 1 < q1;
        constexpr int nprev = !prev_more ? 0 : (q + 1 < 32 ? 6 : 5);
        wait_lgkm<nprev>();
        constexpr int c0 = q < 32 ? 0 : 1;
        constexpr int pa = (q + 3) & 3, ca = q & 3, bb = q % 3;
        if constexpr (q > 0) mma16h(acc[c0][0], A[pa][0], B[bb][c0]); else mma16h(acc[c0][2], A[ca][0], B[bb][c0]);
        if constexpr (more) { A[(q + 2) & 3][0] = afrag(2 * q + 4); }
        if constexpr (q > 0) mma16h(acc[c0][1], A[pa][1], B[bb][c0]); else mma16h(acc[c0][3], A[ca][1], B[bb][c0]);
        if constexpr (more) { A[(q + 2) & 3][1] = afrag(2 * q + 5); }
        if constexpr (q > 0) {
          mma16h(acc[c0][2], A[ca][0], B[bb][c0]);
          if constexpr (more) fetch_b(IC<q + 2>{}, IC<1>{});
          mma16h(acc[c0][3], A[ca][1], B[bb][c0]);
          if constexpr (more) fetch_b(IC<q + 2>{}, IC<0>{});
        } else {
          if constexpr (more) { fetch_b(IC<q + 2>{}, IC<1>{}); fetch_b(IC<q + 2>{}, IC<0>{}); }
        }
        if constexpr (c0 == 0) {
          if constexpr (q > 0) { mma16h(acc[1][0], A[pa][0], B[bb][1]); mma16h(acc[1][1], A[pa][1], B[bb][1]); }
          mma16h(acc[1][2], A[ca][0], B[bb][1]);
          mma16h(acc[1][3], A[ca][1], B[bb][1]);
        }
      });
      MFMA_DRAIN();
    };
    if (hf == 0) run(IC<0>{}, IC<24>{}); else run(IC<24>{}, IC<64>{});
  }
  STAMP(8);
  BAR_LDS();
#pragma unroll
  for (int r = 0; r < 4; ++r) *lds_at<f4>(S_EX + w * 4096 + r * 1024 + l * 16) = sel<f4>(hf, acc[0][r], acc[1][r]);
  const float dsk = bf_us(r_d);
  u2 uu[4];
#pragma unroll
  for (int r = 0; r < 4; ++r) uu[r] = *lds_at<u2>(S_U + chn * 7680 + (16 + 16 * hf + j16) * 160 + (16 * r + 4 * g) * 2);
  BAR_LDS();
  {
    const float isc = *lds_at<float>(S_MISC + 16 + chn * 4);
#pragma unroll
    for (int r = 0; r < 4; ++r) {
      const f4 y = (sel<f4>(hf, acc[1][r], acc[0][r]) + *lds_at<f4>(S_EX + (w ^ 1) * 4096 + r * 1024 + l * 16)) * isc;
      const unsigned u0 = uu[r][0], u1 = uu[r][1];
      const unsigned y0 = pk_bf(y[0], y[1]), y1 = pk_bf(y[2], y[3]);
      const unsigned s0 = pk_bf(h_lo(u0) * dsk, h_hi(u0) * dsk), s1 = pk_bf(h_lo(u1) * dsk, h_hi(u1) * dsk);
      *lds_at<u2>(S_AO + chn * 4096 + (64 * (16 * hf + j16) + 16 * r + 4 * g) * 2) =
          u2{pk_bf(bf_lo(y0) + bf_lo(s0), bf_hi(y0) + bf_hi(s0)), pk_bf(bf_lo(y1) + bf_lo(s1), bf_hi(y1) + bf_hi(s1))};
    }
  }
  BAR_LDS();
#pragma unroll
  for (int it = 0; it < 2; ++it) {
    const u2 c0 = *lds_at<u2>(S_AO + (1024 * it + 4 * tid) * 2), c1 = *lds_at<u2>(S_AO + 4096 + (1024 * it + 4 * tid) * 2);
    u4 o;
    o[0] = __builtin_amdgcn_perm(c1[0], c0[0], 0x05040100u);
    o[1] = __builtin_amdgcn_perm(c1[0], c0[0], 0x07060302u);
    o[2] = __builtin_amdgcn_perm(c1[1], c0[1], 0x05040100u);
    o[3] = __builtin_amdgcn_perm(c1[1], c0[1], 0x07060302u);
    __builtin_amdgcn_raw_buffer_store_b128(o, rs, WS_AT + pair * AT_STRIDE + (1024 * it + 4 * tid) * 4, 0, 16);
  }
  STAMP(9);
  WAIT_VM(0);
  BAR_LDS();
  if (fused && tid == 0) st_flag(p.ws + WS_AFLAG + sb * 8, tag);
  STAMP(10);
  }
}

constexpr unsigned G_SLOT1 = 24576;
constexpr unsigned G_CSP = 0;
constexpr unsigned G_TOT = 65536;
constexpr unsigned G_XR = 2 * G_SLOT1;

__device__ __forceinline__ float gelu_f(float x) {
  const float c0 = -2.f * 0.7978845608028654f * 1.4426950408889634f;
  const float c1 = c0 * 0.044715f;
  const float e = __builtin_amdgcn_exp2f(x * __builtin_fmaf(x * x, c1, c0));
  return x * __builtin_amdgcn_rcpf(1.f + e);
}

struct W2Dma {
  u4 srd; unsigned voff[4]; unsigned lds; int w;
  __device__ __forceinline__ void init(const Params& p, int l, int w_, int nt) {
    srd = make_srd(p.w2, FF * DM * 2);
    w = w_;
    const unsigned v = (l >> 3) * 1024 + (((((l >> 2) & 1) ^ ((l >> 4) & 1)) * 4 + (l & 3)) * 16) + nt * 128;
#pragma unroll
    for (int m = 0; m < 4; ++m) voff[m] = v + m * (8192 - 1024);
    lds = lds_base() + w * 4096;
  }
  __device__ __forceinline__ unsigned soff(int n) const { const int S = w + 4 * n; return (S < 43 ? S : 42) * 32768; }
  __device__ __forceinline__ void m0(int n) const { set_m0(lds + ((n + 1) % 5) * 16384); }
  template <int M> __device__ __forceinline__ void piece(unsigned so) const { dma<M * 1024>(voff[M], srd, so); }
  __device__ __forceinline__ void step(int n) const {
    const unsigned so = soff(n);
    m0(n);
    dma_nop<0>(voff[0], srd, so); piece<1>(so); piece<2>(so); piece<3>(so);
  }
};

struct W2Frag {
  unsigned ab[2][2];
  u4 af[3][2][2];
  __device__ __forceinline__ void init(int l, int w) {
    const int G = l >> 4, q = (l & 15) >> 2, pp = l & 3;
#pragma unroll
    for (int cb = 0; cb < 2; ++cb) {
      ab[0][cb] = (4 * (G >> 1) + q) * 128 + ((cb ^ ((q >> 1) & 1)) * 64) + (G & 1) * 32 + 8 * pp + w * 4096;
      ab[1][cb] = ab[0][cb] + 32768;
    }
  }
  template <int n, int s, int cb> __device__ __forceinline__ void read() {
    constexpr int k = (n + 1) % 5;
    const unsigned o = ab[k >= 2][cb] + (k >= 2 ? k - 2 : k) * 16384 + s * 2048;
    af[n % 3][s][cb] = join(tr_read(o), tr_read(o + 1024));
  }
  template <int n> __device__ __forceinline__ void read_all() { read<n, 0, 0>(); read<n, 0, 1>(); read<n, 1, 0>(); read<n, 1, 1>(); }
};

template <bool fused, bool next>
__device__ __forceinline__ void g_gemm1(const Params& p, int gb, u64& tag, W2Dma& w2d, W2Frag& w2f STAMP_ARG) {
  const int tid = threadIdx.x, w = __builtin_amdgcn_readfirstlane(tid >> 6), l = tid & 63;
  const int xg = gb & 7, j = gb >> 3;
  const int mt1 = 4 * (xg >> 1) + (j >> 3), ct = 8 * (xg & 1) + (j & 7);
  const int sblk = 3 * ct < 2 * ct + 10 ? 3 * ct : 2 * ct + 10;
  const int f0 = 32 * sblk;
  const int tok = l & 31, h = l >> 5, G = l >> 4, q = (l & 15) >> 2, pp = l & 3;
  const int rot = (j + (j >> 3)) & 3;
  const u4 srd_w1 = make_srd(p.w1, DM * FF * 2), srd_x = make_srd(p.x, SEQ * DM * 2), srd_ws = make_srd(p.ws, WS_END);

  unsigned dv[3];
#pragma unroll
  for (int m = 0; m < 3; ++m) {
    const int P = 64 * m + l, row = (P * 171) >> 11, pc = P - row * 12;
    dv[m] = row * (FF * 2) + pc * 16 - m * 1024;
  }
  unsigned dso[4];
#pragma unroll
  for (int c = 0; c < 4; ++c) { dso[c] = (((c + rot) & 3) * 128 + 32 * w) * (FF * 2) + f0 * 2; asm volatile("" : "+s"(dso[c])); }
  const unsigned ldsw = lds_base() + w * 6144;
  auto d_m0 = [&](int c, int half) { set_m0(ldsw + (c & 1) * G_SLOT1 + half * 3072); };
  auto d_piece = [&](auto C, auto I) {
    constexpr int c = C.value, i = I.value;
    dma<(i % 3) * 1024>(dv[i % 3], srd_w1, dso[c] + (i / 3) * 16 * (FF * 2));
  };
  unsigned xv[4];
#pragma unroll
  for (int i = 0; i < 4; ++i)
    xv[i] = (128 * mt1 + 32 * w + 8 * i + (l >> 3)) * 1024 + (((l & 7) ^ (4 * (i & 1) + ((l >> 4) & 3))) * 16) - i * 1024;
  const unsigned ldsx = lds_base() + G_XR + w * 8192;
  auto x_m0 = [&](int cb) { set_m0(ldsx + (cb & 1) * 4096); };
  auto x_so = [&](int cb) -> unsigned { return (((cb >> 1) + rot) & 3) * 256 + (cb & 1) * 128; };
  auto x_piece = [&](auto CB, auto I) { dma<I.value * 1024>(xv[I.value], srd_x, x_so(CB.value)); };

  u2 ep = {0, 0}, bias = {0, 0};
  if constexpr (fused) bld2_sc1<0>(ep, 0u, srd_ws, WS_EPOCH); else bld2<0>(ep, 0u, srd_ws, 0u);
  {
    const u4 srd_b1 = make_srd(p.b1, FF * 2);
    bld2<0>(bias, (f0 + 4 * (l < 24 ? l : 23)) * 2, srd_b1, 0u);
  }
  auto d_half = [&](auto C, auto HF) {
    d_m0(C.value, HF.value);
    asm volatile("s_nop 0" ::: "memory");
    d_piece(C, IC<3 * HF.value>{}); d_piece(C, IC<3 * HF.value + 1>{}); d_piece(C, IC<3 * HF.value + 2>{});
  };
  auto x_block = [&](auto CB) {
    x_m0(CB.value);
    asm volatile("s_nop 0" ::: "memory");
    static_for<0, 4>([&](auto I) { x_piece(CB, I); });
  };
  d_half(IC<0>{}, IC<0>{}); d_half(IC<0>{}, IC<1>{});
  x_block(IC<0>{}); x_block(IC<1>{});
  const auto rs = __builtin_amdgcn_make_buffer_rsrc((void*)p.ws, 0, WS_END, 0x00020000);

  f16v acc[3];
  f4 acs[3];
#pragma unroll
  for (int fb = 0; fb < 3; ++fb) {
#pragma unroll
    for (int r = 0; r < 16; ++r) acc[fb][r] = 0.f;
    acs[fb] = f4{0.f, 0.f, 0.f, 0.f};
    asm volatile("" : "+v"(acc[fb]), "+v"(acs[fb]));
  }
  const unsigned onev = ((G & 1) == ((l >> 3) & 1)) ? 0x3f803f80u : 0u;
  u4 ones = {onev, onev, onev, onev};
  asm volatile("" : "+v"(ones));
  const unsigned ab = (8 * (G >> 1) + q) * 192 + (G & 1) * 32 + 8 * pp;
  u4 af[3][3];
  auto aread = [&](auto S, auto FB) {
    constexpr int s = S.value, fb = FB.value;
    constexpr unsigned o = ((s >> 3) & 1) * G_SLOT1 + (s & 7) * 3072 + fb * 64;
    af[s % 3][fb] = join(tr_read(ab + o), tr_read(ab + o + 768));
  };
  unsigned xr[4];
#pragma unroll
  for (int s = 0; s < 4; ++s) xr[s] = G_XR + w * 8192 + tok * 128 + (((2 * s + h) ^ ((tok >> 1) & 7)) * 16);
  u4 xf[2][4];
  auto xread = [&](auto CB, auto S) { xf[CB.value & 1][S.value] = *lds_at<u4>(xr[S.value] + (CB.value & 1) * 4096); };

  asm volatile("s_waitcnt vmcnt(4)\n\ts_barrier" : "+v"(ep), "+v"(bias)::"memory");
  STAMP(1);
  if constexpr (fused) {
    const unsigned e0 = __builtin_amdgcn_readfirstlane(ep[0]), e1 = __builtin_amdgcn_readfirstlane(ep[1]);
    tag = (((u64)e1 << 32) | e0) + 1;
  }
  static_for<0, 4>([&](auto S) { xread(IC<0>{}, S); });
  SCHED_FENCE();
  static_for<0, 3>([&](auto FB) { aread(IC<0>{}, FB); });
  SCHED_FENCE();
  d_half(IC<1>{}, IC<0>{}); d_half(IC<1>{}, IC<1>{});
  wait_lgkm<6>();
  x_block(IC<2>{});
  static_for<0, 3>([&](auto FB) { aread(IC<1>{}, FB); });
  SCHED_FENCE();
  const unsigned sto = WS_STATS + (128 * mt1 + 32 * w + tok) * 16;

  auto vm_m0 = [&](auto S, auto SL) {
    constexpr int s = S.value, sl = SL.value;
    if constexpr ((s & 3) >= 2 && s < 20 && sl == 0) x_m0((s + 2) / 4 + 2);
    if constexpr ((s == 6 || s == 14) && sl == 2) d_m0(s / 8 + 2, 0);
    if constexpr ((s == 7 || s == 15) && sl == 2) d_m0(s / 8 + 2, 0);
    if constexpr ((s == 8 || s == 16) && sl == 0) d_m0(s / 8 + 1, 0);
    if constexpr ((s == 8 || s == 16) && sl == 1) d_m0(s / 8 + 1, 1);
    if constexpr ((s == 9 || s == 17) && sl == 0) d_m0(s / 8 + 1, 1);
    if constexpr (s == 22 && sl == 0) set_m0(ldsx + 2048);
  };
  auto vm = [&](auto S, auto SL) {
    constexpr int s = S.value, sl = SL.value;
    if constexpr ((s & 3) >= 2 && s < 20 && sl < 2) x_piece(IC<(s + 2) / 4 + 2>{}, IC<2 * (s & 1) + sl>{});
    if constexpr ((s == 6 || s == 14) && sl == 2) d_piece(IC<s / 8 + 2>{}, IC<0>{});
    if constexpr ((s == 7 || s == 15) && sl == 2) d_piece(IC<s / 8 + 2>{}, IC<1>{});
    if constexpr ((s == 8 || s == 16) && sl == 0) d_piece(IC<s / 8 + 1>{}, IC<2>{});
    if constexpr ((s == 8 || s == 16) && sl == 1) d_piece(IC<s / 8 + 1>{}, IC<3>{});
    if constexpr ((s == 9 || s == 17) && sl < 2) d_piece(IC<s / 8 + 1>{}, IC<4 + sl>{});
    if constexpr (s == 22 && sl == 0) dma_sc1(sto, srd_ws, 0u);
  };
  auto step = [&](auto S) {
    constexpr int s = S.value;
    constexpr bool bar = s == 6 || s == 14 || s == 22;
    constexpr bool burst = (s & 3) == 0 && s < 28;
    constexpr bool pburst = ((s - 1) & 3) == 0 && s - 1 < 28;
    constexpr bool more = s + 2 < 32;
    if constexpr (s == 0) wait_vm<10>();
    if constexpr (s == 4) wait_vm<4>();
    if constexpr (s == 8 || s == 16) wait_vm<6>();
    if constexpr (s == 12 || s == 20) wait_vm<9>();
    if constexpr (s == 24) wait_vm<1>();
    if constexpr (s == 6) asm volatile("s_waitcnt vmcnt(8) lgkmcnt(0)\n\ts_barrier" ::: "memory");
    if constexpr (s == 14) asm volatile("s_waitcnt vmcnt(4) lgkmcnt(0)\n\ts_barrier" ::: "memory");
    if constexpr (s == 22) asm volatile("s_waitcnt vmcnt(4) lgkmcnt(0)\n\ts_barrier" ::: "memory");
    if constexpr (bar) wait_lgkm<0>();
    else wait_lgkm<(s == 0 ? 6 : (s == 31 ? 0 : (pburst ? 10 : 6)))>();
    const u4& x = xf[(s >> 2) & 1][s & 3];
    vm_m0(S, IC<0>{});
    mma32(acc[0], af[s % 3][0], x);
    vm(S, IC<0>{});
    if constexpr (more) aread(IC<s + 2>{}, IC<0>{});
    if constexpr (burst) { xread(IC<s / 4 + 1>{}, IC<0>{}); xread(IC<s / 4 + 1>{}, IC<1>{}); }
    vm_m0(S, IC<1>{});
    mma32(acc[1], af[s % 3][1], x);
    vm(S, IC<1>{});
    if constexpr (more) aread(IC<s + 2>{}, IC<1>{});
    if constexpr (burst) { xread(IC<s / 4 + 1>{}, IC<2>{}); xread(IC<s / 4 + 1>{}, IC<3>{}); }
    vm_m0(S, IC<2>{});
    mma32(acc[2], af[s % 3][2], x);
    vm(S, IC<2>{});
    if constexpr (more) aread(IC<s + 2>{}, IC<2>{});
    if (w == (s & 3)) { mma16(acs[0], af[s % 3][0], ones); mma16(acs[1], af[s % 3][1], ones); mma16(acs[2], af[s % 3][2], ones); }
  };
  asm volatile("s_nop 1" ::: "memory");
  static_for<0, 32>(step);
  MFMA_DRAIN();
  STAMP(6);
  if ((l & 7) == 0) {
#pragma unroll
    for (int fb = 0; fb < 3; ++fb) *lds_at<f4>(G_CSP + w * 512 + (fb * 32 + 16 * ((l >> 3) & 1) + 4 * G) * 4) = acs[fb];
  }
  WAIT_VM(0);
  u4 st = *lds_at<u4>(G_XR + w * 8192 + 2048 + l * 16);
  asm volatile("" : "+v"(st));
  BAR_LDS();
  const unsigned tot = G_TOT + w * 4096;
  {
    const int e = l < 24 ? l : 23;
    const f4 c0 = *lds_at<f4>(G_CSP + e * 16), c1 = *lds_at<f4>(G_CSP + 512 + e * 16);
    const f4 c2 = *lds_at<f4>(G_CSP + 1024 + e * 16), c3 = *lds_at<f4>(G_CSP + 1536 + e * 16);
    const unsigned b0 = bias[0], b1 = bias[1];
    *lds_at<f4>(tot + 512 + e * 16) = f4{bf_lo(b0), bf_hi(b0), bf_lo(b1), bf_hi(b1)};
    *lds_at<f4>(tot + e * 16) = (c0 + c1) + (c2 + c3);
  }
  if constexpr (fused) {
    const unsigned tl32 = (unsigned)tag, th32 = (unsigned)(tag >> 32);
    if (!all_lanes(st[1] == tl32 && st[3] == th32)) {
      for (int spins = 0; spins < (1 << 20); ++spins) {
        st = __builtin_amdgcn_raw_buffer_load_b128(rs, sto, 0, 16);
        if (all_lanes(st[1] == tl32 && st[3] == th32)) break;
        __builtin_amdgcn_s_sleep(1);
      }
    }
  }
  f4 csv[12], bbv[12];
#pragma unroll
  for (int gi = 0; gi < 12; ++gi) {
    const unsigned o = tot + ((gi >> 2) * 32 + 16 * ((gi >> 1) & 1) + 8 * (gi & 1) + 4 * h) * 4;
    csv[gi] = *lds_at<f4>(o); bbv[gi] = *lds_at<f4>(o + 512);
  }
  if constexpr (next) BAR_LDS();
  STAMP(5);
  const unsigned st0 = st[0], st2 = st[2];
  const float rho = asf(st2), nmr = -asf(st0) * rho;
  const unsigned hdst = WS_H + ((4 * mt1 + w) * 86 + 2 * sblk) * 1024 + l * 16;
  const unsigned hdst0 = ct < 11 ? hdst : WS_TRASH + gb * 8192 + w * 2048 + l * 16;
  u4 hv, hv0, hv1;
  static_for<0, 12>([&](auto GI) {
    constexpr int gi = GI.value, fb = gi >> 2, s = (gi >> 1) & 1, rg = gi & 1;
    constexpr bool dm = next && gi < 5;
    if constexpr (next && gi == 6) {
      wait_vm<11>();
      w2f.template read_all<0>(); w2f.template read_all<1>(); w2f.template read_all<2>();
    }
    unsigned so = 0;
    if constexpr (dm) { so = w2d.soff(gi); w2d.m0(gi); }
    float v[4];
    static_for<0, 4>([&](auto R) {
      constexpr int r = R.value;
      v[r] = gelu_f(__builtin_fmaf(rho, acc[fb][8 * s + 4 * rg + r], __builtin_fmaf(nmr, csv[gi][r], bbv[gi][r])));
      asm volatile("" : "+v"(v[r]));
      if constexpr (dm) w2d.template piece<r>(so);
    });
    hv[2 * rg] = pk_bf(v[0], v[1]);
    hv[2 * rg + 1] = pk_bf(v[2], v[3]);
    if constexpr (gi == 1) hv0 = hv;
    if constexpr (gi == 3) hv1 = hv;
    if constexpr (gi == 4) {
      __builtin_amdgcn_raw_buffer_store_b128(hv0, rs, hdst0, 0, 16);
      __builtin_amdgcn_raw_buffer_store_b128(hv1, rs, hdst0 + 1024, 0, 16);
    }
    if constexpr (rg == 1 && gi > 3) __builtin_amdgcn_raw_buffer_store_b128(hv, rs, hdst + fb * 2048 + s * 1024, 0, 16);
    SCHED_FENCE();
  });
  STAMP(7);
  WAIT_VM(0);
  asm volatile("s_barrier" ::: "memory");
  if (fused && tid == 0) st_flag(p.ws + WS_HFLAG + gb * 8, tag);
  STAMP(8);
}

template <bool fused, bool waita, bool prefetched>
__device__ __forceinline__ void g_gemm2(const Params& p, int gb, u64& tag, W2Dma& w2d, W2Frag& w2f STAMP_ARG) {
  const int tid = threadIdx.x, w = __builtin_amdgcn_readfirstlane(tid >> 6), l = tid & 63;
  const int xg = gb & 7, j = gb >> 3;
  const int mt2 = 4 * xg + (j >> 3), nt = j & 7;
  const int tok = l & 31, h = l >> 5;
  const u4 srd_ws = make_srd(p.ws, WS_END);

  if constexpr (!prefetched) {
    w2d.step(0); w2d.step(1); w2d.step(2); w2d.step(3); w2d.step(4);
    WAIT_VM(0);
    w2f.template read_all<0>(); w2f.template read_all<1>(); w2f.template read_all<2>();
    SCHED_FENCE();
    wait_lgkm<0>();
    w2d.step(5); w2d.step(6); w2d.step(7);
  } else {
    wait_lgkm<0>();
    w2d.step(5); w2d.step(6); w2d.step(7);
  }
  STAMP(10);
  if (fused) {
    const int mt1 = mt2 >> 1, ct = l & 15;
    const char* fp = p.ws + WS_HFLAG + (2 * (mt1 >> 2) + (ct >> 3) + 8 * (8 * (mt1 & 3) + (ct & 7))) * 8;
    poll_flags(fp, l < 16, tag);
  }
  WAIT_VM(0);
  STAMP(9);
  u4 hf[11][2][2];
  unsigned hv[2];
#pragma unroll
  for (int tb = 0; tb < 2; ++tb) hv[tb] = WS_H + (2 * mt2 + tb) * 86 * 1024 + l * 16;
  auto hso = [&](int n) -> unsigned { const int S = w + 4 * n; return (S < 43 ? S : 42) * 2048; };
  auto hload = [&](auto N, auto S, auto TB) { bld4_sc1<S.value * 1024>(hf[N.value][S.value][TB.value], hv[TB.value], srd_ws, hso(N.value)); };
  static_for<0, 6>([&](auto N) { hload(N, IC<0>{}, IC<0>{}); hload(N, IC<0>{}, IC<1>{}); hload(N, IC<1>{}, IC<0>{}); hload(N, IC<1>{}, IC<1>{}); });

  f16v acc[2][2];
#pragma unroll
  for (int cb = 0; cb < 2; ++cb)
#pragma unroll
    for (int tb = 0; tb < 2; ++tb) {
#pragma unroll
      for (int r = 0; r < 16; ++r) acc[cb][tb][r] = 0.f;
      asm volatile("" : "+v"(acc[cb][tb]));
    }
  const int fcb = w >> 1, ftb = w & 1;
  const int ft = tid >> 3, fc = tid & 7;
  const unsigned go = (64 * mt2 + ft) * 1024 + nt * 128 + fc * 16;
  const unsigned ao = WS_AT + (32 * nt + 16 * fcb + 2 * h) * AT_STRIDE + (64 * mt2 + 32 * ftb + tok) * 4;
  const u4 srd_x = make_srd(p.x, SEQ * DM * 2), srd_b2 = make_srd(p.b2, DM * 2);
  u4 xv0, xv1;
  u2 bv[4], fl = {0, 0};
  unsigned av[4][2];
  bool aok = true;
  auto finld = [&](auto I) {
    constexpr int i = I.value;
    if constexpr (i < 8) bld1_sc1(av[i >> 1][i & 1], ao, srd_ws, (4 * (i >> 1) + (i & 1)) * AT_STRIDE);
    else if constexpr (i < 12) bld2<16 * (i - 8)>(bv[i - 8], (64 * nt + 32 * fcb + 4 * h) * 2, srd_b2, 0u);
    else if constexpr (i == 12) bld4<0>(xv0, go, srd_x, 0u);
    else bld4<0>(xv1, go, srd_x, 32 * 1024);
  };
  asm volatile("s_nop 1" ::: "memory");
  auto step = [&](auto N) {
    constexpr int n = N.value;
    constexpr int W = n <= 2 ? 20 : n == 3 ? 24 : n == 4 ? 28 : n == 5 ? 32 : n == 6 ? 17 : n == 7 ? 10 : n == 8 ? 2 : n == 9 ? 0 : 15;
    constexpr bool rd = n >= 1 && n + 2 <= 10, dm = n >= 2 && n <= 4, hl = n <= 4;
    asm volatile("s_waitcnt vmcnt(%5)" : "+v"(hf[n][0][0]), "+v"(hf[n][0][1]), "+v"(hf[n][1][0]), "+v"(hf[n][1][1]), "+v"(fl) : "i"(W) : "memory");
    wait_lgkm<(n >= 2 && n + 1 <= 10) ? 8 : 0>();
    if constexpr (n == 9 && waita) aok = all_lanes((((u64)fl[1] << 32) | fl[0]) == tag);
    constexpr bool live = n < 10;
    unsigned so = 0;
    if constexpr (dm) so = w2d.soff(n + 6);
    static_for<0, 2>([&](auto S) {
      constexpr int s = S.value;
      if constexpr (live) mma32(acc[0][0], w2f.af[n % 3][s][0], hf[n][s][0]);
      if constexpr (rd) w2f.template read<n + 2, s, 0>();
      if constexpr (n == 9) static_for<7 * s, 7 * s + 4>(finld);
      if constexpr (n == 6 && s == 0) bld2_sc1<0>(fl, (nt + 8 * (l & 31)) * 8, srd_ws, WS_AFLAG);
      if constexpr (live) mma32(acc[1][0], w2f.af[n % 3][s][1], hf[n][s][0]);
      if constexpr (rd) w2f.template read<n + 2, s, 1>();
      if constexpr (n == 9) static_for<7 * s + 4, 7 * s + 7>(finld);
      if constexpr (dm && s == 0) { wait_lgkm<4>(); w2d.m0(n + 6); }
      if constexpr (live) mma32(acc[0][1], w2f.af[n % 3][s][0], hf[n][s][1]);
      if constexpr (dm) w2d.template piece<2 * s>(so);
      if constexpr (hl) hload(IC<n + 6>{}, S, IC<0>{});
      if constexpr (live) mma32(acc[1][1], w2f.af[n % 3][s][1], hf[n][s][1]);
      if constexpr (dm) w2d.template piece<2 * s + 1>(so);
      if constexpr (hl) hload(IC<n + 6>{}, S, IC<1>{});
    });
  };
  static_for<0, 11>(step);
  if (w < 3) {
    static_for<0, 2>([&](auto S) {
      constexpr int s = S.value;
      mma32(acc[0][0], w2f.af[10 % 3][s][0], hf[10][s][0]); mma32(acc[1][0], w2f.af[10 % 3][s][1], hf[10][s][0]);
      mma32(acc[0][1], w2f.af[10 % 3][s][0], hf[10][s][1]); mma32(acc[1][1], w2f.af[10 % 3][s][1], hf[10][s][1]);
    });
    MFMA_DRAIN();
  }
  MFMA_DRAIN();
  STAMP(11);

  BAR_LDS();
  static_for<0, 4>([&](auto D) {
    constexpr int d = D.value;
    if (d != w) {
      const int si = w < d ? w : w - 1;
#pragma unroll
      for (int k = 0; k < 4; ++k) {
        const f16v& a = acc[d >> 1][d & 1];
        *lds_at<f4>((d * 3 + si) * 4096 + k * 1024 + l * 16) = f4{a[4 * k], a[4 * k + 1], a[4 * k + 2], a[4 * k + 3]};
      }
    }
  });
  f16v fin = sel<f16v>(w < 2, sel<f16v>(w == 0, acc[0][0], acc[0][1]), sel<f16v>(w == 2, acc[1][0], acc[1][1]));
  asm volatile("s_waitcnt vmcnt(0)"
               : "+v"(av[0][0]), "+v"(av[0][1]), "+v"(av[1][0]), "+v"(av[1][1]), "+v"(av[2][0]), "+v"(av[2][1]), "+v"(av[3][0]), "+v"(av[3][1]),
                 "+v"(bv[0]), "+v"(bv[1]), "+v"(bv[2]), "+v"(bv[3]), "+v"(xv0), "+v"(xv1)::"memory");
  if (waita && !aok) {
    poll_flags(p.ws + WS_AFLAG + (nt + 8 * (l & 31)) * 8, l < 32, tag);
    const auto rs = __builtin_amdgcn_make_buffer_rsrc((void*)p.ws, 0, WS_END, 0x00020000);
#pragma unroll
    for (int i = 0; i < 8; ++i) av[i >> 1][i & 1] = __builtin_amdgcn_raw_buffer_load_b32(rs, ao + (4 * (i >> 1) + (i & 1)) * AT_STRIDE, 0, 16);
  }
  STAMP(12);
  BAR_LDS();
  constexpr unsigned G_FS = 49152;
#pragma unroll
  for (int k = 0; k < 4; ++k) {
    f4 y = {fin[4 * k], fin[4 * k + 1], fin[4 * k + 2], fin[4 * k + 3]};
#pragma unroll
    for (int si = 0; si < 3; ++si) y += *lds_at<f4>((w * 3 + si) * 4096 + k * 1024 + l * 16);
    const unsigned b0 = bv[k][0], b1 = bv[k][1], a0 = av[k][0], a1 = av[k][1];
    y += f4{bf_lo(b0) + bf_lo(a0), bf_hi(b0) + bf_hi(a0), bf_lo(b1) + bf_lo(a1), bf_hi(b1) + bf_hi(a1)};
    *lds_at<f4>(G_FS + (32 * ftb + tok) * 272 + (32 * fcb + 8 * k + 4 * h) * 4) = y;
  }
  BAR_LDS();
  {
    const unsigned o = G_FS + ft * 272 + fc * 32;
    const f4 a0 = *lds_at<f4>(o), a1 = *lds_at<f4>(o + 16), b0 = *lds_at<f4>(o + 32 * 272), b1 = *lds_at<f4>(o + 32 * 272 + 16);
    u4 o0, o1;
    o0[0] = pk_bf(bf_lo(xv0[0]) + a0[0], bf_hi(xv0[0]) + a0[1]); o0[1] = pk_bf(bf_lo(xv0[1]) + a0[2], bf_hi(xv0[1]) + a0[3]);
    o0[2] = pk_bf(bf_lo(xv0[2]) + a1[0], bf_hi(xv0[2]) + a1[1]); o0[3] = pk_bf(bf_lo(xv0[3]) + a1[2], bf_hi(xv0[3]) + a1[3]);
    o1[0] = pk_bf(bf_lo(xv1[0]) + b0[0], bf_hi(xv1[0]) + b0[1]); o1[1] = pk_bf(bf_lo(xv1[1]) + b0[2], bf_hi(xv1[1]) + b0[3]);
    o1[2] = pk_bf(bf_lo(xv1[2]) + b1[0], bf_hi(xv1[2]) + b1[1]); o1[3] = pk_bf(bf_lo(xv1[3]) + b1[2], bf_hi(xv1[3]) + b1[3]);
    *(u4*)(p.out + go) = o0;
    *(u4*)(p.out + go + 32 * 1024) = o1;
  }
}

template <int MODE>
__global__ void __launch_bounds__(256, 2) fused_kernel(Params p) {
  const int b = blockIdx.x;
  constexpr bool fused = (MODE & 16) != 0;
  STAMP(0);
  u64 tag = 0;
#define GPRIO 0
#define SPRIO 0
  if (b >= 256) {
    const int gb = b - 256;
    __builtin_amdgcn_s_setprio(GPRIO);
    W2Dma w2d;
    w2d.init(p, threadIdx.x & 63, __builtin_amdgcn_readfirstlane(threadIdx.x >> 6), (gb >> 3) & 7);
    W2Frag w2f;
    w2f.init(threadIdx.x & 63, __builtin_amdgcn_readfirstlane(threadIdx.x >> 6));
    if constexpr ((MODE & 2) != 0) g_gemm1<fused, (MODE & 8) != 0>(p, gb, tag, w2d, w2f STAMP_PASS);
    if constexpr ((MODE & 8) != 0) g_gemm2<fused, fused && !(MODE & 32), (MODE & 2) != 0>(p, gb, tag, w2d, w2f STAMP_PASS);
  } else {
    const int sb = b;
    __builtin_amdgcn_s_setprio(SPRIO);
    if constexpr ((MODE & 5) != 0) s_role<fused, (MODE & 1) != 0, (MODE & 4) != 0>(p, sb, tag STAMP_PASS);
    if (fused && sb == 0) {
      const int tid = threadIdx.x;
      poll_flags(p.ws + WS_XNFLAG + tid * 8, true, tag);
      poll_flags(p.ws + WS_HFLAG + tid * 8, true, tag);
      BAR_LDS();
      if (tid == 0) st_flag(p.ws + WS_EPOCH, tag);
    }
  }
}

#if defined(__HIP_DEVICE_COMPILE__)
#pragma clang attribute pop
#endif
}

extern "C" void kernel_launch(void* const* d_in, const int* in_sizes, int n_in, void* d_out, int out_size,
                              void* d_ws, size_t ws_size, hipStream_t stream) {
  (void)in_sizes; (void)n_in; (void)out_size; (void)ws_size;
  Params p;
  p.x = (const char*)d_in[0];
  p.logA = (const unsigned short*)d_in[1];
  p.Aim = (const unsigned short*)d_in[2];
  p.Cre = (const unsigned short*)d_in[3];
  p.Cim = (const unsigned short*)d_in[4];
  p.logdt = (const unsigned short*)d_in[5];
  p.dskip = (const unsigned short*)d_in[6];
  p.w1 = (const char*)d_in[8];
  p.b1 = (const char*)d_in[9];
  p.w2 = (const char*)d_in[10];
  p.b2 = (const char*)d_in[11];
  p.out = (char*)d_out;
  p.ws = (char*)d_ws;
#define LAUNCH(M) do { (void)hipFuncSetAttribute((const void*)fused_kernel<M>, hipFuncAttributeMaxDynamicSharedMemorySize, LDS_BYTES); \
    fused_kernel<M><<<512, 256, LDS_BYTES, stream>>>(p); } while (0)
  LAUNCH(31);
}
```

```cpp
#include <hip/hip_runtime.h>
#include <hip/hip_bf16.h>
#include <stdint.h>

namespace {
#if defined(__HIP_DEVICE_COMPILE__)
#pragma clang attribute push(__attribute__((target("no-packed-fp32-ops"))), apply_to = function)
#endif

typedef float f2 __attribute__((ext_vector_type(2)));
typedef float f4 __attribute__((ext_vector_type(4)));
typedef float f16v __attribute__((ext_vector_type(16)));
typedef unsigned u2 __attribute__((ext_vector_type(2)));
typedef unsigned u4 __attribute__((ext_vector_type(4)));
typedef short s4 __attribute__((ext_vector_type(4)));
typedef _Float16 h2 __attribute__((ext_vector_type(2)));
typedef __bf16 b2 __attribute__((ext_vector_type(2)));
typedef unsigned long long u64;
#define LAS __attribute__((address_space(3)))
#define GAS __attribute__((address_space(1)))
typedef GAS u64 gu64;

constexpr int SEQ = 2048, DM = 512, NST = 128, FF = 1376;

constexpr unsigned WS_EPOCH = 0;
constexpr unsigned WS_XNFLAG = 4096;
constexpr unsigned WS_HFLAG = 4096 + 2048;
constexpr unsigned WS_AFLAG = 4096 + 4096;
constexpr unsigned WS_STATS = 16384;
constexpr unsigned XNB_STRIDE = 8192 + 128, AT_STRIDE = 8192 + 128;
constexpr unsigned WS_XNB = 65536;
constexpr unsigned WS_AT = WS_XNB + 256 * XNB_STRIDE;
constexpr unsigned WS_H = WS_AT + 256 * AT_STRIDE;
constexpr unsigned WS_TRASH = WS_H + 64 * 86 * 1024;
constexpr unsigned WS_END = WS_TRASH + 256 * 8192;

constexpr int LDS_BYTES = 81920;

struct Params {
  const char* x;
  const unsigned short* logA;
  const unsigned short* Aim;
  const unsigned short* Cre;
  const unsigned short* Cim;
  const unsigned short* logdt;
  const unsigned short* dskip;
  const char* w1;
  const char* b1;
  const char* w2;
  const char* b2;
  char* out;
  char* ws;
};

__device__ __forceinline__ float bf_lo(unsigned u) { return __builtin_bit_cast(float, u << 16); }
__device__ __forceinline__ float bf_hi(unsigned u) { return __builtin_bit_cast(float, u & 0xffff0000u); }
__device__ __forceinline__ float bf_us(unsigned short u) { return __builtin_bit_cast(float, (unsigned)u << 16); }
__device__ __forceinline__ unsigned pk_bf(float a, float b) { b2 v = {(__bf16)a, (__bf16)b}; return __builtin_bit_cast(unsigned, v); }
__device__ __forceinline__ unsigned pk_h(float a, float b) { h2 v = {(_Float16)a, (_Float16)b}; return __builtin_bit_cast(unsigned, v); }
__device__ __forceinline__ float bfr(float a) { return bf_lo(pk_bf(a, a)); }
__device__ __forceinline__ float asf(unsigned u) { return __builtin_bit_cast(float, u); }
__device__ __forceinline__ unsigned asu(float u) { return __builtin_bit_cast(unsigned, u); }
__device__ __forceinline__ float h_lo(unsigned u) { return (float)__builtin_bit_cast(h2, u)[0]; }
__device__ __forceinline__ float h_hi(unsigned u) { return (float)__builtin_bit_cast(h2, u)[1]; }

template <int CTRL>
__device__ __forceinline__ float dpp_add(float x) {
  return x + asf((unsigned)__builtin_amdgcn_update_dpp(0, (int)asu(x), CTRL, 0xf, 0xf, true));
}
__device__ __forceinline__ float sum16(float x) {
  x = dpp_add<0x128>(x); x = dpp_add<0x124>(x); x = dpp_add<0x122>(x); x = dpp_add<0x121>(x);
  return x;
}
__device__ __forceinline__ float swap16_add(float a, float b) {
  auto r = __builtin_amdgcn_permlane16_swap(asu(a), asu(b), false, false);
  return asf(r[0]) + asf(r[1]);
}
__device__ __forceinline__ float swap32_add(float a, float b) {
  auto r = __builtin_amdgcn_permlane32_swap(asu(a), asu(b), false, false);
  return asf(r[0]) + asf(r[1]);
}
__device__ __forceinline__ float sum32(float x) { x = sum16(x); return swap16_add(x, x); }
__device__ __forceinline__ float sum64(float x) { x = sum32(x); return swap32_add(x, x); }

struct cplx { float x, y, ny; };
__device__ __forceinline__ f2 cmul(const cplx& a, f2 b) {
  return f2{__builtin_fmaf(a.ny, b.y, a.x * b.x), __builtin_fmaf(a.y, b.x, a.x * b.y)};
}
__device__ __forceinline__ cplx cx(f2 v) { return cplx{v.x, v.y, -v.y}; }

#define BAR_LDS() asm volatile("s_waitcnt lgkmcnt(0)\n\ts_barrier" ::: "memory")
#define WAIT_VM(n) asm volatile("s_waitcnt vmcnt(" #n ")" ::: "memory")
#define SCHED_FENCE() __builtin_amdgcn_sched_barrier(0)
template <int N> __device__ __forceinline__ void wait_vm() { asm volatile("s_waitcnt vmcnt(%0)" ::"i"(N) : "memory"); }
template <int N> __device__ __forceinline__ void wait_lgkm() { asm volatile("" ::: "memory"); __builtin_amdgcn_s_waitcnt(0xC07F | (N << 8)); asm volatile("" ::: "memory"); }

template <int N> struct IC { static constexpr int value = N; };
template <int B, int E, typename Fn>
__device__ __forceinline__ void static_for(Fn&& fn) {
  if constexpr (B < E) { fn(IC<B>{}); static_for<B + 1, E>(fn); }
}

__device__ __forceinline__ bool all_lanes(bool c) { return __builtin_amdgcn_ballot_w64(!c) == 0; }
__device__ __forceinline__ float exp_acc(float x) {
  const float L = 1.44269502162933349609375f, Llo = 1.925963033500011e-8f;
  const float t = x * L, r = __builtin_fmaf(x, Llo, __builtin_fmaf(x, L, -t));
  const float e = __builtin_amdgcn_exp2f(t);
  return __builtin_fmaf(e, r * 0.6931471805599453f, e);
}
__device__ __forceinline__ u64 ld_flag(const char* p) {
  return __hip_atomic_load((gu64*)p, __ATOMIC_RELAXED, __HIP_MEMORY_SCOPE_AGENT);
}
__device__ __forceinline__ void st_flag(char* p, u64 v) {
  __hip_atomic_store((gu64*)p, v, __ATOMIC_RELAXED, __HIP_MEMORY_SCOPE_AGENT);
}
template <int SLEEP = 4>
__device__ __forceinline__ void poll_flags(const char* p, bool active, u64 tag) {
  for (int spins = 0; spins < (1 << 20); ++spins) {
    u64 v = tag;
    if (active) v = ld_flag(p);
    if (all_lanes(v == tag)) break;
    __builtin_amdgcn_s_sleep(SLEEP);
  }
}

template <typename T>
__device__ __forceinline__ LAS T* lds_at(unsigned off) {
  extern __shared__ __attribute__((aligned(16))) char smem[];
  return (LAS T*)((LAS char*)smem + off);
}
__device__ __forceinline__ unsigned lds_base() {
  extern __shared__ __attribute__((aligned(16))) char smem[];
  return (unsigned)(size_t)(LAS char*)smem;
}
__device__ __forceinline__ u2 tr_read(unsigned off) {
  return __builtin_bit_cast(u2, __builtin_amdgcn_ds_read_tr16_b64_v4i16(lds_at<s4>(off)));
}
template <typename T> __device__ __forceinline__ T sel(bool c, T a, T b) { return c ? a : b; }
__device__ __forceinline__ u4 join(u2 lo, u2 hi) { return u4{lo[0], lo[1], hi[0], hi[1]}; }

__device__ __forceinline__ u4 make_srd(const void* p, unsigned bytes) {
  const u64 a = (u64)p;
  return u4{(unsigned)a, (unsigned)(a >> 32) & 0xffffu, bytes, 0x00020000u};
}
__device__ __forceinline__ void set_m0(unsigned v) { asm volatile("s_mov_b32 m0, %0" ::"s"(v) : "memory"); }
template <int IMM>
__device__ __forceinline__ void dma(unsigned voff, const u4& srd, unsigned soff) {
  asm volatile("buffer_load_dwordx4 %0, %1, %2 offen offset:%3 lds" ::"v"(voff), "s"(srd), "s"(soff), "i"(IMM) : "memory");
}
__device__ __forceinline__ void dma_sc1(unsigned voff, const u4& srd, unsigned soff) {
  asm volatile("buffer_load_dwordx4 %0, %1, %2 offen sc1 lds" ::"v"(voff), "s"(srd), "s"(soff) : "memory");
}
template <int IMM>
__device__ __forceinline__ void bld4(u4& d, unsigned voff, const u4& srd, unsigned soff) {
  asm volatile("buffer_load_dwordx4 %0, %1, %2, %3 offen offset:%4" : "=v"(d) : "v"(voff), "s"(srd), "s"(soff), "i"(IMM) : "memory");
}
template <int IMM>
__device__ __forceinline__ void bld4_sc1(u4& d, unsigned voff, const u4& srd, unsigned soff) {
  asm volatile("buffer_load_dwordx4 %0, %1, %2, %3 offen offset:%4 sc1" : "=v"(d) : "v"(voff), "s"(srd), "s"(soff), "i"(IMM) : "memory");
}
template <int IMM>
__device__ __forceinline__ void bld2(u2& d, unsigned voff, const u4& srd, unsigned soff) {
  asm volatile("buffer_load_dwordx2 %0, %1, %2, %3 offen offset:%4" : "=v"(d) : "v"(voff), "s"(srd), "s"(soff), "i"(IMM) : "memory");
}
template <int IMM>
__device__ __forceinline__ void bld2_sc1(u2& d, unsigned voff, const u4& srd, unsigned soff) {
  asm volatile("buffer_load_dwordx2 %0, %1, %2, %3 offen offset:%4 sc1" : "=v"(d) : "v"(voff), "s"(srd), "s"(soff), "i"(IMM) : "memory");
}
__device__ __forceinline__ void bld1_sc1(unsigned& d, unsigned voff, const u4& srd, unsigned soff) {
  asm volatile("buffer_load_dword %0, %1, %2, %3 offen sc1" : "=v"(d) : "v"(voff), "s"(srd), "s"(soff) : "memory");
}
__device__ __forceinline__ void touch(const char* base, unsigned off, unsigned lds_sink) {
  asm volatile("s_mov_b32 m0, %2\n\ts_nop 0\n\tglobal_load_lds_dword %0, %1" ::"v"(off), "s"(base), "s"(lds_sink) : "memory");
}
__device__ __forceinline__ void touch_m0(const u4& srd, unsigned off) {
  asm volatile("buffer_load_dword %0, %1, 0 offen lds" ::"v"(off), "s"(srd) : "memory");
}

__device__ __forceinline__ void mma32(f16v& c, const u4& a, const u4& b) {
  asm volatile("v_mfma_f32_32x32x16_bf16 %0, %1, %2, %0" : "+v"(c) : "v"(a), "v"(b) : "memory");
}
__device__ __forceinline__ void mma16(f4& c, const u4& a, const u4& b) {
  asm volatile("v_mfma_f32_16x16x32_bf16 %0, %1, %2, %0" : "+v"(c) : "v"(a), "v"(b) : "memory");
}
__device__ __forceinline__ void mma16h(f4& c, const u4& a, const u4& b) {
  asm volatile("v_mfma_f32_16x16x32_f16 %0, %1, %2, %0" : "+v"(c) : "v"(a), "v"(b) : "memory");
}
#define MFMA_DRAIN() asm volatile("s_nop 7\n\ts_nop 7" ::: "memory")

constexpr int PRIO_GEMM1 = 2, PRIO_REST = 0;

constexpr unsigned S_TB = 0;
constexpr unsigned S_TA = 17408;
constexpr unsigned S_R = 70720;
constexpr unsigned S_STAGE = 70720;
constexpr unsigned S_MISC = 79424;
constexpr unsigned S_SINK = 79488;
constexpr unsigned S_DUMMY = 79744;
constexpr unsigned S_CP = 0;
constexpr unsigned S_U = 35328;
constexpr unsigned S_EX = 0;
constexpr unsigned S_AO = 16384;

template <bool fused, bool do_ln, bool do_conv>
__device__ __forceinline__ void s_role(const Params& p, int sb, u64& tag) {
  const int tid = threadIdx.x, w = __builtin_amdgcn_readfirstlane(tid >> 6), l = tid & 63;
  const int chn = w >> 1, hf = w & 1;
  const int pair = 32 * (sb & 7) + (sb >> 3);
  const int ch = 2 * pair + chn;
  const int j16 = l & 15, g = l >> 4;
  const auto rs = __builtin_amdgcn_make_buffer_rsrc((void*)p.ws, 0, WS_END, 0x00020000);

  u4 xa = {0, 0, 0, 0}, xb = {0, 0, 0, 0};
  const int blk = 32 * (sb & 7) + (sb >> 3);
  const int c = l & 31, tl = 2 * w + (l >> 5), t = 8 * blk + tl;
  if constexpr (do_ln) {
    const u4* xr = (const u4*)(p.x + (size_t)t * 1024);
    xa = xr[c]; xb = xr[32 + c];
  }
  if constexpr (fused) tag = ld_flag(p.ws + WS_EPOCH) + 1;
  unsigned short r_dt = 0, r_la = 0, r_ai = 0, r_cr = 0, r_ci = 0, r_d = 0;
  const int pi = ch * NST + 64 * hf + l;
  if constexpr (do_conv) {
    r_dt = p.logdt[ch]; r_la = p.logA[pi]; r_ai = p.Aim[pi]; r_cr = p.Cre[pi]; r_ci = p.Cim[pi];
    r_d = p.dskip[ch];
  }

  if constexpr (do_ln) {
    float v[16];
#pragma unroll
    for (int i = 0; i < 4; ++i) {
      v[2 * i] = bf_lo(xa[i]); v[2 * i + 1] = bf_hi(xa[i]);
      v[8 + 2 * i] = bf_lo(xb[i]); v[8 + 2 * i + 1] = bf_hi(xb[i]);
    }
    float s0 = v[0] + v[8], s1 = v[1] + v[9];
#pragma unroll
    for (int i = 2; i < 8; i += 2) { s0 += v[i]; s1 += v[i + 1]; s0 += v[8 + i]; s1 += v[9 + i]; }
    const float mean = sum32(s0 + s1) * (1.f / 512.f);
    float q0 = 0.f, q1 = 0.f;
#pragma unroll
    for (int i = 0; i < 16; i += 2) { const float d0 = v[i] - mean, d1 = v[i + 1] - mean; q0 = __builtin_fmaf(d0, d0, q0); q1 = __builtin_fmaf(d1, d1, q1); }
    const float var = sum32(q0 + q1) * (1.f / 512.f);
    const float m = bfr(mean);
    const float sd = bfr(__builtin_amdgcn_sqrtf(bfr(bfr(var) + 1e-5f)));
    const float r1 = __builtin_amdgcn_rcpf(sd), mr = -m * r1;
    unsigned hx[8];
#pragma unroll
    for (int i = 0; i < 8; ++i) hx[i] = pk_h(__builtin_fmaf(v[2 * i], r1, mr), __builtin_fmaf(v[2 * i + 1], r1, mr));
    *lds_at<u4>(S_STAGE + tl * 1088 + c * 16) = u4{hx[0], hx[1], hx[2], hx[3]};
    *lds_at<u4>(S_STAGE + tl * 1088 + 512 + c * 16) = u4{hx[4], hx[5], hx[6], hx[7]};
    const float m2 = bfr((mean - m) * r1);
    const float sd2 = bfr(__builtin_amdgcn_sqrtf(bfr(bfr(var * r1 * r1) + 1e-5f)));
    const float mu = __builtin_fmaf(m2, sd, m);
    const float rho = __builtin_amdgcn_rcpf(sd * sd2);
    if (c == 0)
      __builtin_amdgcn_raw_buffer_store_b128(u4{asu(mu), (unsigned)tag, asu(rho), (unsigned)(tag >> 32)}, rs, WS_STATS + t * 16, 0, 16);
    BAR_LDS();
#pragma unroll
    for (int ps = 0; ps < 2; ++ps) {
      const int cb = 128 * w + 64 * ps + 16 * g;
      const int q = j16 >> 2, pp = l & 3;
      const u2 lo = tr_read(S_STAGE + q * 1088 + (cb + 4 * pp) * 2);
      const u2 hi = tr_read(S_STAGE + (4 + q) * 1088 + (cb + 4 * pp) * 2);
      __builtin_amdgcn_raw_buffer_store_b128(join(lo, hi), rs, WS_XNB + blk * XNB_STRIDE + (128 * w + 64 * ps + l) * 16, 0, 16);
    }
    if constexpr (!do_conv) { WAIT_VM(0); BAR_LDS(); if (fused && tid == 0) st_flag(p.ws + WS_XNFLAG + blk * 8, tag); }
  }
  if constexpr (do_conv) {
  const float dt = bfr(exp_acc(bf_us(r_dt)));
  const float ar = -bfr(exp_acc(bf_us(r_la)));
  const float zr = ar * dt, zi = bf_us(r_ai) * dt;
  const float cr = bf_us(r_cr) * dt, ci = bf_us(r_ci) * dt;
  const float bound = sum64(__builtin_fabsf(cr) + __builtin_fabsf(ci));
  const int eb = __builtin_amdgcn_frexp_expf(bound);
  const float sA = __builtin_amdgcn_ldexpf(1.f, 8 - eb), isA = __builtin_amdgcn_ldexpf(1.f, eb - 8);
  const f2 C = {cr * sA, ci * sA};
  const float a2 = zr * 1.44269504088896341f, b2r = zi * 0.15915494309189535f;
  auto direct = [&](float e) -> f2 {
    const float mag = __builtin_amdgcn_exp2f(a2 * e);
    const float fr = __builtin_amdgcn_fractf(b2r * e);
    return f2{mag * __builtin_amdgcn_cosf(fr), mag * __builtin_amdgcn_sinf(fr)};
  };
  const unsigned tb = S_TB + w * (16 * 272) + l * 4, ta = S_TA + w * (41 * 272) + l * 4;
  auto putB = [&](int row, f2 v) { *lds_at<unsigned>(tb + row * 272) = pk_h(v.x, v.y); };
  auto putA = [&](int row, f2 v) { *lds_at<unsigned>(ta + row * 272) = pk_h(v.x, -v.y); };

  const f2 P1 = direct(1.f), P2 = direct(2.f), P4 = direct(4.f), P8 = direct(8.f), P16 = direct(16.f), P32 = direct(32.f), P64 = direct(64.f);
  const f2 P128 = direct(128.f), P256 = direct(256.f), P512 = direct(512.f), P1024 = direct(1024.f);
  {
    const cplx c16 = cx(P16), c32 = cx(P32), c64 = cx(P64);
    f2 B[8];
    B[0] = f2{1.f, 0.f}; B[1] = P8; B[2] = P16; B[3] = cmul(c16, P8); B[4] = P32;
    B[5] = cmul(c32, P8); B[6] = cmul(c32, P16); B[7] = cmul(c32, B[3]);
#pragma unroll
    for (int j = 0; j < 8; ++j) putB(j, B[j]);
    putB(8, P64);
#pragma unroll
    for (int j = 1; j < 8; ++j) putB(8 + j, cmul(c64, B[j]));
  }
  if constexpr (do_ln) {
    WAIT_VM(0);
    asm volatile("s_barrier" ::: "memory");
    if (fused && tid == 0) st_flag(p.ws + WS_XNFLAG + blk * 8, tag);
  }
  {
    const cplx cC = cx(C), c2 = cx(P2), c4 = cx(P4), c128 = cx(P128), c256 = cx(P256), c512 = cx(P512);
    f2 Q[8];
    const f2 P3 = cmul(c2, P1);
    Q[0] = C; Q[1] = cmul(cC, P1); Q[2] = cmul(cC, P2); Q[3] = cmul(cC, P3); Q[4] = cmul(cC, P4);
    { const cplx q4 = cx(Q[4]); Q[5] = cmul(q4, P1); Q[6] = cmul(q4, P2); Q[7] = cmul(q4, P3); }
#pragma unroll
    for (int b = 0; b < 8; ++b) { putA(b, Q[b]); putA(8 + b, cmul(c128, Q[b])); }
#pragma unroll
    for (int b = 0; b < 4; ++b) { const f2 t = cmul(c256, Q[2 * b]); putA(16 + b, t); putA(20 + b, cmul(c128, t)); }
    const f2 P384 = cmul(c256, P128);
    const cplx c384 = cx(P384);
#pragma unroll
    for (int b = 0; b < 2; ++b) {
      const f2 u = cmul(c512, Q[4 * b]);
      putA(24 + b, u); putA(26 + b, cmul(c128, u)); putA(28 + b, cmul(c256, u)); putA(30 + b, cmul(c384, u));
    }
    const f2 V = cmul(cC, P1024);
    const cplx cV = cx(V);
    putA(32, V); putA(33, cmul(cV, P128)); putA(34, cmul(cV, P256)); putA(35, cmul(cV, P384)); putA(36, cmul(cV, P512));
    putA(37, cmul(cV, cmul(c512, P128))); putA(38, cmul(cV, cmul(c512, P256))); putA(39, cmul(cV, cmul(c512, P384)));
    putA(40, cmul(cV, P1024));
  }
  const char* xfp = p.ws + WS_XNFLAG + (32 * w + (l & 31) + 128 * (l >> 5)) * 8;
  u64 xfl = tag;
  if constexpr (fused) xfl = ld_flag(xfp);
  f4 D1 = {0.f, 0.f, 0.f, 0.f}, D2 = D1, D3 = D1;
  {
    typedef _Float16 h8 __attribute__((ext_vector_type(8)));
    const unsigned rb = S_TB + w * (16 * 272) + j16 * 272 + g * 16, ra = S_TA + w * (41 * 272) + j16 * 272 + g * 16;
    const unsigned ra3 = S_TA + w * (41 * 272) + (32 + (j16 < 8 ? j16 : 8)) * 272 + g * 16;
    h8 fb[4], f1[4], f2_[4], f3[4];
#pragma unroll
    for (int s = 0; s < 4; ++s) {
      fb[s] = *lds_at<h8>(rb + s * 64); f1[s] = *lds_at<h8>(ra + s * 64);
      f2_[s] = *lds_at<h8>(ra + 16 * 272 + s * 64); f3[s] = *lds_at<h8>(ra3 + s * 64);
    }
    SCHED_FENCE();
#pragma unroll
    for (int s = 0; s < 4; ++s) {
      D1 = __builtin_amdgcn_mfma_f32_16x16x32_f16(f1[s], fb[s], D1, 0, 0, 0);
      D2 = __builtin_amdgcn_mfma_f32_16x16x32_f16(f2_[s], fb[s], D2, 0, 0, 0);
      D3 = __builtin_amdgcn_mfma_f32_16x16x32_f16(fb[s], f3[s], D3, 0, 0, 0);
    }
  }
  {
    const unsigned ex = S_TA + w * (41 * 272);
    *lds_at<f4>(ex + l * 16) = sel<f4>(hf, D1, D2) * isA;
    if (hf) *lds_at<f4>(ex + 1024 + l * 16) = D3 * isA;
    if (l == 0) *lds_at<float>(S_MISC + w * 4) = bound;
  }
  u4 ua = {0, 0, 0, 0}, ub = {0, 0, 0, 0};
  const unsigned usrc = WS_XNB + (tid >> 1) * XNB_STRIDE + pair * 32 + (tid & 1) * 16;
  const bool early = all_lanes(xfl == tag);
  if (early) {
    ua = __builtin_amdgcn_raw_buffer_load_b128(rs, usrc, 0, 16);
    ub = __builtin_amdgcn_raw_buffer_load_b128(rs, usrc + 128 * XNB_STRIDE, 0, 16);
  }
  if (tid < 2 * 64) {
    const int c2 = tid >> 6, e = tid & 63;
    *lds_at<unsigned short>(S_R + c2 * 4224 + (e < 12 ? e : 2048 + e) * 2) = 0;
  }
  BAR_LDS();
  {
    const unsigned ex = S_TA + (w ^ 1) * (41 * 272);
    const f4 o0 = *lds_at<f4>(ex + l * 16);
    f4 o1 = D3;
    if (!hf) o1 = *lds_at<f4>(ex + 1024 + l * 16);
    const float btot = 2.f * (bound + *lds_at<float>(S_MISC + (w ^ 1) * 4));
    const int et = __builtin_amdgcn_frexp_expf(btot);
    const float sc = __builtin_amdgcn_ldexpf(2.f, 15 - et);
    const unsigned rb0 = S_R + chn * 4224;
    auto st64 = [&](unsigned a, unsigned v0, unsigned v1) { *lds_at<u2>(a) = u2{v0, v1}; };
    auto st32 = [&](unsigned a, unsigned v) { *lds_at<unsigned>(a) = v; };
    auto st16 = [&](unsigned a, unsigned v) { *lds_at<unsigned short>(a) = (unsigned short)v; };
    if (hf == 0) {
      float a[4], b[4];
#pragma unroll
      for (int r = 0; r < 4; ++r) { a[r] = __builtin_fmaf(D1[r], isA, o0[r]) * sc; b[r] = __builtin_fmaf(D3[r], isA, o1[r]) * sc; }
      st64(rb0 + 2 * (2056 - 128 * (g >> 1) - 4 * (g & 1) - 8 * j16), pk_h(a[3], a[2]), pk_h(a[1], a[0]));
      const int p0 = 1024 + 128 * j16 + 32 * g;
      const unsigned a0 = j16 < 8 ? rb0 + 2 * (2032 - p0) : S_DUMMY;
      const unsigned k0 = pk_h(b[0], b[0]), k1 = pk_h(b[1], b[1]), k2 = pk_h(b[2], b[2]), k3 = pk_h(b[3], b[3]);
      *lds_at<u4>(a0) = u4{k3, k3, k3, pk_h(b[3], b[2])};
      *lds_at<u4>(a0 + 16) = u4{k2, k2, k2, k2};
      *lds_at<u4>(a0 + 32) = u4{k1, k1, k1, pk_h(b[1], b[0])};
      st64(a0 + 48, k0, k0); st32(a0 + 56, k0);
      st32((l == 0 ? S_DUMMY : a0) + 60, k0);
      if (l == 8) st64(rb0 + 24, k0, k0);
    } else {
      float a[4];
#pragma unroll
      for (int r = 0; r < 4; ++r) a[r] = __builtin_fmaf(D2[r], isA, o0[r]) * sc;
      const unsigned k0 = pk_h(a[0], a[0]), k1 = pk_h(a[1], a[1]), k2 = pk_h(a[2], a[2]), k3 = pk_h(a[3], a[3]);
      if (g >= 2) {
        const unsigned e = rb0 + 2 * (2054 - (512 + 256 * (g - 2) + 8 * j16));
        st32(e, k1); st32(e + 4, pk_h(a[1], a[0])); st32(e + 8, k0); st32(e + 12, k0);
        st32(e - 256, k3); st32(e - 252, pk_h(a[3], a[2])); st32(e - 248, k2); st32(e - 244, k2);
      }
      asm volatile("" ::: "memory");
      if (g < 2) {
        const unsigned e = rb0 + 2 * (2053 - (256 + 128 * g + 8 * j16));
        st16(e, k3); st32(e + 2, k2); st32(e + 6, pk_h(a[2], a[1])); st32(e + 10, k0);
        st16(l == 0 ? S_DUMMY : e + 14, k0);
      }
    }
    if (l == 0 && hf == 0) *lds_at<float>(S_MISC + 16 + chn * 4) = __builtin_amdgcn_ldexpf(1.f, et - 15);
  }
  BAR_LDS();
#pragma unroll
  for (int it = 0; it < 3; ++it) {
    int c2, Y;
    if (it < 2) { c2 = it; Y = tid; } else { c2 = tid >> 3; Y = 256 + (tid & 7); }
    if (it < 2 || (tid < 16 && (tid & 7) < 5)) {
      const u4 lo = *lds_at<u4>(S_R + c2 * 4224 + Y * 16 + 16), hi = *lds_at<u4>(S_R + c2 * 4224 + Y * 16 + 32);
      const unsigned o0 = __builtin_amdgcn_alignbit(lo[1], lo[0], 16), o1 = __builtin_amdgcn_alignbit(lo[2], lo[1], 16);
      const unsigned o2 = __builtin_amdgcn_alignbit(lo[3], lo[2], 16), o3 = __builtin_amdgcn_alignbit(hi[0], lo[3], 16);
      const unsigned o4 = __builtin_amdgcn_alignbit(hi[1], hi[0], 16);
      const unsigned dst = S_CP + c2 * 17664 + Y * 16;
      *lds_at<u4>(dst) = u4{o1, o2, o3, o4};
      *lds_at<u4>(dst + 4416) = u4{lo[1], lo[2], lo[3], hi[0]};
      *lds_at<u4>(dst + 2 * 4416) = u4{o0, o1, o2, o3};
      *lds_at<u4>(dst + 3 * 4416) = lo;
    }
  }
  if (tid < 160) { *lds_at<u4>(S_U + tid * 16) = u4{0, 0, 0, 0}; *lds_at<u4>(S_U + 7680 + tid * 16) = u4{0, 0, 0, 0}; }
  if (!early) {
    poll_flags(xfp, true, tag);
    ua = __builtin_amdgcn_raw_buffer_load_b128(rs, usrc, 0, 16);
    ub = __builtin_amdgcn_raw_buffer_load_b128(rs, usrc + 128 * XNB_STRIDE, 0, 16);
  }
  {
    const int bk = tid >> 1;
    const unsigned dst = S_U + (tid & 1) * 7680 + (16 + (bk >> 3)) * 160 + (bk & 7) * 16;
    *lds_at<u4>(dst) = ua;
    *lds_at<u4>(dst + 16 * 160) = ub;
  }
  BAR_LDS();
  {
    constexpr int K0 = 512;
    constexpr int nl = (FF - K0) * 8;
    constexpr int per = (nl + 127) / 128;
    const int i0 = ((sb >> 3) * 4 + w) * per;
#pragma unroll
    for (int k = 0; k < per; k += 64) {
      const int i = i0 + k + l;
      touch(p.w2, (unsigned)(K0 * 1024) + (unsigned)((k + l < per && i < nl) ? i : 0) * 128, lds_base() + S_SINK);
    }
  }
  f4 acc[2][4];
  {
    const int a = j16 >> 2, b = j16 & 3;
    const unsigned abase = S_CP + chn * 17664 + b * 4416 + (2048 - 16 * 127 - 4 * a + 8 * g) * 2;
    const unsigned ubase = S_U + chn * 7680 + j16 * 160 + g * 16;
    auto afrag = [&](int ww) -> u4 {
      const u2 lo = *lds_at<volatile u2>(abase + (127 - ww) * 32), hi = *lds_at<volatile u2>(abase + (127 - ww) * 32 + 8);
      return join(lo, hi);
    };
    auto bfrag = [&](int ct, int q) -> u4 {
      const int off = (16 + 16 * ct) * 160 + ((q & 1) ? -160 * ((q - 1) / 2) : -160 * (q / 2) + 64);
      return *lds_at<u4>(ubase + off);
    };
    u4 A[4][2], B[3][2];
    auto run = [&](auto Q0, auto Q1) {
      constexpr int q0 = Q0.value, q1 = Q1.value;
#pragma unroll
      for (int ct = 0; ct < 2; ++ct)
#pragma unroll
        for (int r = 0; r < 4; ++r) { acc[ct][r] = f4{0.f, 0.f, 0.f, 0.f}; asm volatile("" : "+v"(acc[ct][r])); }
      auto fetch_a = [&](auto Q) { constexpr int q = Q.value; A[q & 3][0] = afrag(2 * q); A[q & 3][1] = afrag(2 * q + 1); };
      auto fetch_b = [&](auto Q, auto CT) { constexpr int q = Q.value, ct = CT.value; if constexpr (ct == 1 || q < 32) B[q % 3][ct] = bfrag(ct, q); };
      if constexpr (q0 > 0) fetch_a(IC<q0 - 1>{});
      fetch_a(IC<q0>{}); fetch_b(IC<q0>{}, IC<0>{}); fetch_b(IC<q0>{}, IC<1>{});
      SCHED_FENCE();
      fetch_a(IC<q0 + 1>{}); fetch_b(IC<q0 + 1>{}, IC<0>{}); fetch_b(IC<q0 + 1>{}, IC<1>{});
      SCHED_FENCE();
      asm volatile("s_nop 1" ::: "memory");
      static_for<q0, q1>([&](auto Q) {
        constexpr int q = Q.value;
        constexpr bool more = q + 2 < q1, prev_more = q + 1 < q1;
        constexpr int nprev = !prev_more ? 0 : (q + 1 < 32 ? 6 : 5);
        wait_lgkm<nprev>();
        constexpr int c0 = q < 32 ? 0 : 1;
        constexpr int pa = (q + 3) & 3, ca = q & 3, bb = q % 3;
        if constexpr (q > 0) mma16h(acc[c0][0], A[pa][0], B[bb][c0]); else mma16h(acc[c0][2], A[ca][0], B[bb][c0]);
        if constexpr (more) { A[(q + 2) & 3][0] = afrag(2 * q + 4); }
        if constexpr (q > 0) mma16h(acc[c0][1], A[pa][1], B[bb][c0]); else mma16h(acc[c0][3], A[ca][1], B[bb][c0]);
        if constexpr (more) { A[(q + 2) & 3][1] = afrag(2 * q + 5); }
        if constexpr (q > 0) {
          mma16h(acc[c0][2], A[ca][0], B[bb][c0]);
          if constexpr (more) fetch_b(IC<q + 2>{}, IC<1>{});
          mma16h(acc[c0][3], A[ca][1], B[bb][c0]);
          if constexpr (more) fetch_b(IC<q + 2>{}, IC<0>{});
        } else {
          if constexpr (more) { fetch_b(IC<q + 2>{}, IC<1>{}); fetch_b(IC<q + 2>{}, IC<0>{}); }
        }
        if constexpr (c0 == 0) {
          if constexpr (q > 0) { mma16h(acc[1][0], A[pa][0], B[bb][1]); mma16h(acc[1][1], A[pa][1], B[bb][1]); }
          mma16h(acc[1][2], A[ca][0], B[bb][1]);
          mma16h(acc[1][3], A[ca][1], B[bb][1]);
        }
      });
      MFMA_DRAIN();
    };
    if (hf == 0) run(IC<0>{}, IC<24>{}); else run(IC<24>{}, IC<64>{});
  }
  if (hf) {
#pragma unroll
    for (int r = 0; r < 4; ++r) { const f4 t = acc[0][r]; acc[0][r] = acc[1][r]; acc[1][r] = t; }
  }
  BAR_LDS();
#pragma unroll
  for (int r = 0; r < 4; ++r) *lds_at<f4>(S_EX + w * 4096 + r * 1024 + l * 16) = acc[1][r];
  const float dsk = bf_us(r_d);
  u2 uu[4];
#pragma unroll
  for (int r = 0; r < 4; ++r) uu[r] = *lds_at<u2>(S_U + chn * 7680 + (16 + 16 * hf + j16) * 160 + (16 * r + 4 * g) * 2);
  const float isc = *lds_at<float>(S_MISC + 16 + chn * 4);
  SCHED_FENCE();
  BAR_LDS();
  {
    f4 o[4];
#pragma unroll
    for (int r = 0; r < 4; ++r) o[r] = *lds_at<f4>(S_EX + (w ^ 1) * 4096 + r * 1024 + l * 16);
    SCHED_FENCE();
#pragma unroll
    for (int r = 0; r < 4; ++r) {
      const f4 y = (acc[0][r] + o[r]) * isc;
      const unsigned u0 = uu[r][0], u1 = uu[r][1];
      *lds_at<u2>(S_AO + chn * 4608 + (16 * hf + j16) * 144 + (16 * r + 4 * g) * 2) =
          u2{pk_bf(__builtin_fmaf(h_lo(u0), dsk, y[0]), __builtin_fmaf(h_hi(u0), dsk, y[1])),
             pk_bf(__builtin_fmaf(h_lo(u1), dsk, y[2]), __builtin_fmaf(h_hi(u1), dsk, y[3]))};
    }
  }
  BAR_LDS();
  {
    u2 c0[2], c1[2];
#pragma unroll
    for (int it = 0; it < 2; ++it) {
      const unsigned o = S_AO + (16 * it + (tid >> 4)) * 144 + (tid & 15) * 8;
      c0[it] = *lds_at<u2>(o); c1[it] = *lds_at<u2>(o + 4608);
    }
    SCHED_FENCE();
#pragma unroll
    for (int it = 0; it < 2; ++it) {
      u4 o;
      o[0] = __builtin_amdgcn_perm(c1[it][0], c0[it][0], 0x05040100u);
      o[1] = __builtin_amdgcn_perm(c1[it][0], c0[it][0], 0x07060302u);
      o[2] = __builtin_amdgcn_perm(c1[it][1], c0[it][1], 0x05040100u);
      o[3] = __builtin_amdgcn_perm(c1[it][1], c0[it][1], 0x07060302u);
      __builtin_amdgcn_raw_buffer_store_b128(o, rs, WS_AT + pair * AT_STRIDE + (1024 * it + 4 * tid) * 4, 0, 16);
    }
  }
  WAIT_VM(0);
  BAR_LDS();
  if (fused && tid == 0) st_flag(p.ws + WS_AFLAG + sb * 8, tag);
  }
}

constexpr unsigned G_SLOT1 = 24576;
constexpr unsigned G_CSP = 0;
constexpr unsigned G_XR = 2 * G_SLOT1;

__device__ __forceinline__ float gelu_f(float x) {
  const float c0 = -2.f * 0.7978845608028654f * 1.4426950408889634f;
  const float c1 = c0 * 0.044715f;
  const float e = __builtin_amdgcn_exp2f(x * __builtin_fmaf(x * x, c1, c0));
  return x * __builtin_amdgcn_rcpf(1.f + e);
}

struct W2Dma {
  u4 srd; unsigned voff[4]; unsigned lds; int w;
  __device__ __forceinline__ void init(const Params& p, int l, int w_, int nt) {
    srd = make_srd(p.w2, FF * DM * 2);
    w = w_;
    const unsigned v = (l >> 3) * 1024 + (((((l >> 2) & 1) ^ ((l >> 4) & 1)) * 4 + (l & 3)) * 16) + nt * 128;
#pragma unroll
    for (int m = 0; m < 4; ++m) voff[m] = v + m * (8192 - 1024);
    lds = lds_base() + w * 4096;
  }
  __device__ __forceinline__ unsigned soff(int n) const { const int S = w + 4 * n; return (S < 43 ? S : 42) * 32768; }
  __device__ __forceinline__ void m0(int n) const { set_m0(lds + ((n + 1) % 5) * 16384); }
  template <int M> __device__ __forceinline__ void piece(unsigned so) const { dma<M * 1024>(voff[M], srd, so); }
  __device__ __forceinline__ void step(int n) const {
    const unsigned so = soff(n);
    m0(n);
    asm volatile("s_nop 0" ::: "memory");
    piece<0>(so); piece<1>(so); piece<2>(so); piece<3>(so);
  }
};

struct W2Frag {
  unsigned ab[2][2];
  u4 af[3][2][2];
  __device__ __forceinline__ void init(int l, int w) {
    const int G = l >> 4, q = (l & 15) >> 2, pp = l & 3;
#pragma unroll
    for (int a = 0; a < 2; ++a) {
      const int cb = a ^ (w >> 1);
      ab[0][a] = (4 * (G >> 1) + q) * 128 + ((cb ^ ((q >> 1) & 1)) * 64) + (G & 1) * 32 + 8 * pp + w * 4096;
      ab[1][a] = ab[0][a] + 32768;
    }
  }
  template <int n, int s, int cb> __device__ __forceinline__ void read() {
    constexpr int k = (n + 1) % 5;
    const unsigned o = ab[k >= 2][cb] + (k >= 2 ? k - 2 : k) * 16384 + s * 2048;
    af[n % 3][s][cb] = join(tr_read(o), tr_read(o + 1024));
  }
  template <int n> __device__ __forceinline__ void read_all() { read<n, 0, 0>(); read<n, 0, 1>(); read<n, 1, 0>(); read<n, 1, 1>(); }
};

template <bool fused, bool next>
__device__ __forceinline__ void g_gemm1(const Params& p, int gb, u64& tag, W2Dma& w2d, W2Frag& w2f) {
  const int tid = threadIdx.x, w = __builtin_amdgcn_readfirstlane(tid >> 6), l = tid & 63;
  const int xg = gb & 7, j = gb >> 3;
  const int mt1 = 4 * (xg >> 1) + (j >> 3), ct = 8 * (xg & 1) + (j & 7);
  const int sblk = 3 * ct < 2 * ct + 10 ? 3 * ct : 2 * ct + 10;
  const int f0 = 32 * sblk;
  const int tok = l & 31, h = l >> 5, G = l >> 4, q = (l & 15) >> 2, pp = l & 3;
  const int rot = (j + (j >> 3)) & 3;
  const u4 srd_x = make_srd(p.x, SEQ * DM * 2);
  unsigned xv[2][4];
#pragma unroll
  for (int b = 0; b < 2; ++b)
#pragma unroll
    for (int i = 0; i < 4; ++i)
      xv[b][i] = (128 * mt1 + 32 * w + 8 * i + (l >> 3)) * 1024 +
                 ((128 * b + 64 * w + ((l & 7) ^ (4 * (i & 1) + ((l >> 4) & 3))) * 16) & 255) - i * 1024;
  const unsigned ldsx = lds_base() + G_XR + w * 8192;
  auto x_m0 = [&](int cb) { set_m0(ldsx + (cb & 1) * 4096); };
  auto x_so = [&](int cb) -> unsigned { return (((cb >> 1) + rot) & 3) * 256; };
  auto x_piece = [&](auto CB, auto I) { dma<I.value * 1024>(xv[CB.value & 1][I.value], srd_x, x_so(CB.value)); };

  auto x_block = [&](auto CB) {
    x_m0(CB.value);
    asm volatile("s_nop 0" ::: "memory");
    static_for<0, 4>([&](auto I) { x_piece(CB, I); });
  };
  x_block(IC<0>{});
  SCHED_FENCE();
  const u4 srd_w1 = make_srd(p.w1, DM * FF * 2), srd_ws = make_srd(p.ws, WS_END);
  unsigned dv[3];
#pragma unroll
  for (int m = 0; m < 3; ++m) {
    const int P = 64 * m + l, row = (P * 171) >> 11, pc = P - row * 12;
    dv[m] = row * (FF * 2) + pc * 16 - m * 1024;
  }
  unsigned dso[4];
#pragma unroll
  for (int c = 0; c < 4; ++c) { dso[c] = (((c + rot) & 3) * 128 + 32 * w) * (FF * 2) + f0 * 2; asm volatile("" : "+s"(dso[c])); }
  const unsigned ldsw = lds_base() + w * 6144;
  auto d_m0 = [&](int c, int half) { set_m0(ldsw + (c & 1) * G_SLOT1 + half * 3072); };
  auto d_piece = [&](auto C, auto I) {
    constexpr int c = C.value, i = I.value;
    dma<(i % 3) * 1024>(dv[i % 3], srd_w1, dso[c] + (i / 3) * 16 * (FF * 2));
  };
  auto d_half = [&](auto C, auto HF) {
    d_m0(C.value, HF.value);
    asm volatile("s_nop 0" ::: "memory");
    d_piece(C, IC<3 * HF.value>{}); d_piece(C, IC<3 * HF.value + 1>{}); d_piece(C, IC<3 * HF.value + 2>{});
  };
  d_half(IC<0>{}, IC<0>{}); d_half(IC<0>{}, IC<1>{});
  u2 ep = {0, 0}, bias = {0, 0};
  if constexpr (fused) bld2_sc1<0>(ep, 0u, srd_ws, WS_EPOCH); else bld2<0>(ep, 0u, srd_ws, 0u);
  {
    const u4 srd_b1 = make_srd(p.b1, FF * 2);
    bld2<0>(bias, (f0 + 4 * (l < 24 ? l : 23)) * 2, srd_b1, 0u);
  }
  x_block(IC<1>{});
  const auto rs = __builtin_amdgcn_make_buffer_rsrc((void*)p.ws, 0, WS_END, 0x00020000);

  f16v acc[3];
  f4 acs[3];
#pragma unroll
  for (int fb = 0; fb < 3; ++fb) {
#pragma unroll
    for (int r = 0; r < 16; ++r) acc[fb][r] = 0.f;
    acs[fb] = f4{0.f, 0.f, 0.f, 0.f};
    asm volatile("" : "+v"(acc[fb]), "+v"(acs[fb]));
  }
  const unsigned onev = ((G & 1) == ((l >> 3) & 1)) ? 0x3f803f80u : 0u;
  u4 ones = {onev, onev, onev, onev};
  asm volatile("" : "+v"(ones));
  unsigned ab[8];
#pragma unroll
  for (int i = 0; i < 8; ++i) ab[i] = (8 * (G >> 1) + q) * 192 + (G & 1) * 32 + 8 * pp + ((i + 2 * w) & 7) * 3072;
  u4 af[3][3];
  auto aread = [&](auto S, auto FB) {
    constexpr int s = S.value, fb = FB.value;
    constexpr unsigned o = ((s >> 3) & 1) * G_SLOT1 + fb * 64;
    af[s % 3][fb] = join(tr_read(ab[s & 7] + o), tr_read(ab[s & 7] + o + 768));
  };
  unsigned xr[4];
#pragma unroll
  for (int s = 0; s < 4; ++s) xr[s] = G_XR + w * 8192 + tok * 128 + (((2 * s + h) ^ ((tok >> 1) & 7)) * 16);
  u4 xf[2][4];
  auto xread = [&](auto CB, auto S) { xf[CB.value & 1][S.value] = *lds_at<u4>(xr[S.value] + (CB.value & 1) * 4096); };

  asm volatile("s_waitcnt vmcnt(4)\n\ts_barrier" : "+v"(ep), "+v"(bias)::"memory");
  if constexpr (fused) {
    const unsigned e0 = __builtin_amdgcn_readfirstlane(ep[0]), e1 = __builtin_amdgcn_readfirstlane(ep[1]);
    tag = (((u64)e1 << 32) | e0) + 1;
  }
  static_for<0, 4>([&](auto S) { xread(IC<0>{}, S); });
  SCHED_FENCE();
  static_for<0, 3>([&](auto FB) { aread(IC<0>{}, FB); });
  SCHED_FENCE();
  d_half(IC<1>{}, IC<0>{}); d_half(IC<1>{}, IC<1>{});
  wait_lgkm<6>();
  x_block(IC<2>{});
  static_for<0, 3>([&](auto FB) { aread(IC<1>{}, FB); });
  SCHED_FENCE();
  const unsigned sto = WS_STATS + (128 * mt1 + 32 * w + tok) * 16;

  const u4 srd_w2t = make_srd(p.w2, FF * DM * 2);
  auto vm_m0 = [&](auto S, auto SL) {
    constexpr int s = S.value, sl = SL.value;
    if constexpr ((s & 3) >= 2 && s < 20 && sl == 0) x_m0((s + 2) / 4 + 2);
    if constexpr ((s == 6 || s == 14) && sl == 2) d_m0(s / 8 + 2, 0);
    if constexpr ((s == 7 || s == 15) && sl == 2) d_m0(s / 8 + 2, 0);
    if constexpr ((s == 8 || s == 16) && sl == 0) d_m0(s / 8 + 1, 0);
    if constexpr ((s == 8 || s == 16) && sl == 1) d_m0(s / 8 + 1, 1);
    if constexpr ((s == 9 || s == 17) && sl == 0) d_m0(s / 8 + 1, 1);
    if constexpr (s == 22 && sl == 0) set_m0(ldsx + 2048);
    if constexpr (next && s == 25 && sl == 0) set_m0(lds_base() + w * 4096 + 2048);
  };
  auto vm = [&](auto S, auto SL) {
    constexpr int s = S.value, sl = SL.value;
    if constexpr ((s & 3) >= 2 && s < 20 && sl < 2) x_piece(IC<(s + 2) / 4 + 2>{}, IC<2 * (s & 1) + sl>{});
    if constexpr ((s == 6 || s == 14) && sl == 2) d_piece(IC<s / 8 + 2>{}, IC<0>{});
    if constexpr ((s == 7 || s == 15) && sl == 2) d_piece(IC<s / 8 + 2>{}, IC<1>{});
    if constexpr ((s == 8 || s == 16) && sl == 0) d_piece(IC<s / 8 + 1>{}, IC<2>{});
    if constexpr ((s == 8 || s == 16) && sl == 1) d_piece(IC<s / 8 + 1>{}, IC<3>{});
    if constexpr ((s == 9 || s == 17) && sl < 2) d_piece(IC<s / 8 + 1>{}, IC<4 + sl>{});
    if constexpr (s == 22 && sl == 0) dma_sc1(sto, srd_ws, 0u);
    if constexpr (next && s == 25 && sl == 0) touch_m0(srd_w2t, (160 * (j >> 3) + 40 * w + (l < 40 ? l : 39)) * 1024 + (j & 7) * 128);
  };
  const unsigned tot = G_XR + w * 8192;
  f4 cp[4];
  f4 csv[12], bbv[12];
  u4 st;
  auto step = [&](auto S) {
    constexpr int s = S.value;
    constexpr bool bar = s == 6 || s == 14 || s == 22 || s == 28;
    constexpr bool burst = (s & 3) == 0 && s < 28;
    constexpr bool pburst = ((s - 1) & 3) == 0 && s - 1 < 28;
    constexpr bool more = s + 2 < 32;
    if constexpr (s == 0) wait_vm<10>();
    if constexpr (s == 4) wait_vm<4>();
    if constexpr (s == 8 || s == 16) wait_vm<6>();
    if constexpr (s == 12 || s == 20) wait_vm<9>();
    if constexpr (s == 24) wait_vm<1>();
    if constexpr (s == 6) asm volatile("s_waitcnt vmcnt(8) lgkmcnt(0)\n\ts_barrier" ::: "memory");
    if constexpr (s == 14) asm volatile("s_waitcnt vmcnt(4) lgkmcnt(0)\n\ts_barrier" ::: "memory");
    if constexpr (s == 22) asm volatile("s_waitcnt vmcnt(4) lgkmcnt(0)\n\ts_barrier" ::: "memory");
    if constexpr (s == 28) { if constexpr (next) asm volatile("s_waitcnt vmcnt(1) lgkmcnt(0)\n\ts_barrier" ::: "memory"); else asm volatile("s_waitcnt vmcnt(0) lgkmcnt(0)\n\ts_barrier" ::: "memory"); }
    if constexpr (bar) wait_lgkm<0>();
    else wait_lgkm<(s == 0 ? 6 : (s == 31 ? 0 : (pburst ? 10 : 6)))>();
    const u4& x = xf[(s >> 2) & 1][s & 3];
    vm_m0(S, IC<0>{});
    mma32(acc[0], af[s % 3][0], x);
    vm(S, IC<0>{});
    if constexpr (more) aread(IC<s + 2>{}, IC<0>{});
    if constexpr (s == 27) {
      if ((l & 7) == 0) {
#pragma unroll
        for (int fb = 0; fb < 3; ++fb) *lds_at<f4>(G_CSP + w * 512 + (fb * 32 + 16 * ((l >> 3) & 1) + 4 * G) * 4) = acs[fb];
      }
    }
    if constexpr (s == 28) {
      const int e = l < 24 ? l : 23;
#pragma unroll
      for (int i = 0; i < 4; ++i) cp[i] = *lds_at<f4>(G_CSP + i * 512 + e * 16);
      st = *lds_at<u4>(G_XR + w * 8192 + 2048 + l * 16);
    }
    if constexpr (s == 31) {
#pragma unroll
      for (int gi = 0; gi < 12; ++gi) {
        const unsigned o = tot + ((gi >> 2) * 32 + 16 * ((gi >> 1) & 1) + 8 * (gi & 1) + 4 * h) * 4;
        csv[gi] = *lds_at<f4>(o); bbv[gi] = *lds_at<f4>(o + 512);
      }
    }
    if constexpr (s == 29) {
      const int e = l < 24 ? l : 23;
      const unsigned b0 = bias[0], b1 = bias[1];
      *lds_at<f4>(tot + 512 + e * 16) = f4{bf_lo(b0), bf_hi(b0), bf_lo(b1), bf_hi(b1)};
      *lds_at<f4>(tot + e * 16) = (cp[0] + cp[1]) + (cp[2] + cp[3]);
    }
    if constexpr (burst) { xread(IC<s / 4 + 1>{}, IC<0>{}); xread(IC<s / 4 + 1>{}, IC<1>{}); }
    vm_m0(S, IC<1>{});
    mma32(acc[1], af[s % 3][1], x);
    vm(S, IC<1>{});
    if constexpr (more) aread(IC<s + 2>{}, IC<1>{});
    if constexpr (burst) { xread(IC<s / 4 + 1>{}, IC<2>{}); xread(IC<s / 4 + 1>{}, IC<3>{}); }
    vm_m0(S, IC<2>{});
    mma32(acc[2], af[s % 3][2], x);
    vm(S, IC<2>{});
    if constexpr (more) aread(IC<s + 2>{}, IC<2>{});
    if constexpr ((s & 7) < 2) { mma16(acs[0], af[s % 3][0], ones); mma16(acs[1], af[s % 3][1], ones); mma16(acs[2], af[s % 3][2], ones); }
  };
  asm volatile("s_nop 1" ::: "memory");
  static_for<0, 32>(step);
  MFMA_DRAIN();
  if constexpr (next) { w2d.init(p, l, w, j & 7); w2f.init(l, w); }
  if constexpr (fused) {
    const unsigned tl32 = (unsigned)tag, th32 = (unsigned)(tag >> 32);
    if (!all_lanes(st[1] == tl32 && st[3] == th32)) {
      for (int spins = 0; spins < (1 << 20); ++spins) {
        st = __builtin_amdgcn_raw_buffer_load_b128(rs, sto, 0, 16);
        if (all_lanes(st[1] == tl32 && st[3] == th32)) break;
        __builtin_amdgcn_s_sleep(1);
      }
    }
  }
  if constexpr (next) BAR_LDS();
  __builtin_amdgcn_s_setprio(PRIO_REST);
  const unsigned st0 = st[0], st2 = st[2];
  const float rho = asf(st2), nmr = -asf(st0) * rho;
  const unsigned hdst = WS_H + ((4 * mt1 + w) * 86 + 2 * sblk) * 1024 + l * 16;
  const unsigned hdst0 = ct < 11 ? hdst : WS_TRASH + gb * 8192 + w * 2048 + l * 16;
  u4 hv, hv0, hv1;
  static_for<0, 12>([&](auto GI) {
    constexpr int gi = GI.value, fb = gi >> 2, s = (gi >> 1) & 1, rg = gi & 1;
    constexpr bool dm = next && gi < 5;
    if constexpr (next && gi == 6) {
      wait_vm<11>();
      w2f.template read_all<0>(); w2f.template read_all<1>(); w2f.template read_all<2>();
    }
    unsigned so = 0;
    if constexpr (dm) { so = w2d.soff(gi); w2d.m0(gi); }
    float v[4];
    static_for<0, 4>([&](auto R) {
      constexpr int r = R.value;
      v[r] = gelu_f(__builtin_fmaf(rho, acc[fb][8 * s + 4 * rg + r], __builtin_fmaf(nmr, csv[gi][r], bbv[gi][r])));
      asm volatile("" : "+v"(v[r]));
      if constexpr (dm) w2d.template piece<r>(so);
    });
    hv[2 * rg] = pk_bf(v[0], v[1]);
    hv[2 * rg + 1] = pk_bf(v[2], v[3]);
    if constexpr (gi == 1) hv0 = hv;
    if constexpr (gi == 3) hv1 = hv;
    if constexpr (gi == 4) {
      __builtin_amdgcn_raw_buffer_store_b128(hv0, rs, hdst0, 0, 16);
      __builtin_amdgcn_raw_buffer_store_b128(hv1, rs, hdst0 + 1024, 0, 16);
    }
    if constexpr (rg == 1 && gi > 3) __builtin_amdgcn_raw_buffer_store_b128(hv, rs, hdst + fb * 2048 + s * 1024, 0, 16);
    SCHED_FENCE();
  });
  WAIT_VM(0);
  asm volatile("s_barrier" ::: "memory");
  if (fused && tid == 0) st_flag(p.ws + WS_HFLAG + gb * 8, tag);
}

template <bool fused, bool waita, bool prefetched>
__device__ __forceinline__ void g_gemm2(const Params& p, int gb, u64& tag, W2Dma& w2d, W2Frag& w2f) {
  const int tid = threadIdx.x, w = __builtin_amdgcn_readfirstlane(tid >> 6), l = tid & 63;
  const int xg = gb & 7, j = gb >> 3;
  const int mt2 = 4 * xg + (j >> 3), nt = j & 7;
  const int tok = l & 31, h = l >> 5;
  const u4 srd_ws = make_srd(p.ws, WS_END);

  if constexpr (!prefetched) {
    w2d.init(p, l, w, nt); w2f.init(l, w);
    w2d.step(0); w2d.step(1); w2d.step(2); w2d.step(3); w2d.step(4);
    WAIT_VM(0);
    w2f.template read_all<0>(); w2f.template read_all<1>(); w2f.template read_all<2>();
    SCHED_FENCE();
    wait_lgkm<0>();
    w2d.step(5); w2d.step(6); w2d.step(7);
  } else {
    wait_lgkm<0>();
    w2d.step(5); w2d.step(6); w2d.step(7);
  }
  const int fcb = w >> 1, ftb = w & 1;
  const int ft = tid >> 3, fc = tid & 7;
  const unsigned go = (64 * mt2 + ft) * 1024 + nt * 128 + fc * 16;
  u4 xv0, xv1;
  u2 bv[4];
  {
    const u4 srd_x = make_srd(p.x, SEQ * DM * 2), srd_b2 = make_srd(p.b2, DM * 2);
    bld4<0>(xv0, go, srd_x, 0u); bld4<0>(xv1, go, srd_x, 32 * 1024);
    const unsigned bo2 = (64 * nt + 32 * fcb + 4 * h) * 2;
    bld2<0>(bv[0], bo2, srd_b2, 0u); bld2<16>(bv[1], bo2, srd_b2, 0u); bld2<32>(bv[2], bo2, srd_b2, 0u); bld2<48>(bv[3], bo2, srd_b2, 0u);
  }
  if (fused) {
    const int mt1 = mt2 >> 1, ct = l & 15;
    const char* fp = p.ws + WS_HFLAG + (2 * (mt1 >> 2) + (ct >> 3) + 8 * (8 * (mt1 & 3) + (ct & 7))) * 8;
    poll_flags<1>(fp, l < 16, tag);
  }
  asm volatile("s_waitcnt vmcnt(0)" : "+v"(xv0), "+v"(xv1), "+v"(bv[0]), "+v"(bv[1]), "+v"(bv[2]), "+v"(bv[3])::"memory");
  u4 hf[11][2][2];
  unsigned hv[2];
#pragma unroll
  for (int b = 0; b < 2; ++b) hv[b] = WS_H + (2 * mt2 + (b ^ (w & 1))) * 86 * 1024 + l * 16;
  auto hso = [&](int n) -> unsigned { const int S = w + 4 * n; return (S < 43 ? S : 42) * 2048; };
  auto hload = [&](auto N, auto S, auto TB) { bld4_sc1<S.value * 1024>(hf[N.value][S.value][TB.value], hv[TB.value], srd_ws, hso(N.value)); };
  static_for<0, 6>([&](auto N) { hload(N, IC<0>{}, IC<0>{}); hload(N, IC<0>{}, IC<1>{}); hload(N, IC<1>{}, IC<0>{}); hload(N, IC<1>{}, IC<1>{}); });

  f16v acc[2][2];
#pragma unroll
  for (int cb = 0; cb < 2; ++cb)
#pragma unroll
    for (int tb = 0; tb < 2; ++tb) {
#pragma unroll
      for (int r = 0; r < 16; ++r) acc[cb][tb][r] = 0.f;
      asm volatile("" : "+v"(acc[cb][tb]));
    }
  const unsigned ao = WS_AT + (32 * nt + 16 * fcb + 2 * h) * AT_STRIDE + (64 * mt2 + 32 * ftb + tok) * 4;
  u2 fl = {0, 0};
  unsigned av[4][2];
  bool aok = true;
  auto finld = [&](auto I) {
    constexpr int i = I.value;
    bld1_sc1(av[i >> 1][i & 1], ao, srd_ws, (4 * (i >> 1) + (i & 1)) * AT_STRIDE);
  };
  asm volatile("s_nop 1" ::: "memory");
  auto step = [&](auto N) {
    constexpr int n = N.value;
    constexpr int W = n <= 2 ? 20 : n == 3 ? 24 : n == 4 ? 28 : n == 5 ? 32 : n == 6 ? 17 : n == 7 ? 10 : n == 8 ? 2 : n == 9 ? 0 : 9;
    constexpr bool rd = n >= 1 && n + 2 <= 10, dm = n >= 2 && n <= 4, hl = n <= 4;
    asm volatile("s_waitcnt vmcnt(%5)" : "+v"(hf[n][0][0]), "+v"(hf[n][0][1]), "+v"(hf[n][1][0]), "+v"(hf[n][1][1]), "+v"(fl) : "i"(W) : "memory");
    wait_lgkm<(n >= 2 && n + 1 <= 10) ? 8 : 0>();
    if constexpr (n == 9 && waita) aok = all_lanes((((u64)fl[1] << 32) | fl[0]) == tag);
    constexpr bool live = n < 10;
    unsigned so = 0;
    if constexpr (dm) so = w2d.soff(n + 6);
    static_for<0, 2>([&](auto S) {
      constexpr int s = S.value;
      if constexpr (live) mma32(acc[0][0], w2f.af[n % 3][s][0], hf[n][s][0]);
      if constexpr (rd) w2f.template read<n + 2, s, 0>();
      if constexpr (n == 9) static_for<4 * s, 4 * s + 2>(finld);
      if constexpr (n == 6 && s == 0) bld2_sc1<0>(fl, (nt + 8 * (l & 31)) * 8, srd_ws, WS_AFLAG);
      if constexpr (live) mma32(acc[1][0], w2f.af[n % 3][s][1], hf[n][s][0]);
      if constexpr (rd) w2f.template read<n + 2, s, 1>();
      if constexpr (n == 9) static_for<4 * s + 2, 4 * s + 4>(finld);
      if constexpr (dm && s == 0) { wait_lgkm<4>(); w2d.m0(n + 6); }
      if constexpr (live) mma32(acc[0][1], w2f.af[n % 3][s][0], hf[n][s][1]);
      if constexpr (dm) w2d.template piece<2 * s>(so);
      if constexpr (hl) hload(IC<n + 6>{}, S, IC<0>{});
      if constexpr (live) mma32(acc[1][1], w2f.af[n % 3][s][1], hf[n][s][1]);
      if constexpr (dm) w2d.template piece<2 * s + 1>(so);
      if constexpr (hl) hload(IC<n + 6>{}, S, IC<1>{});
    });
  };
  static_for<0, 11>(step);
  if (w < 3) {
    static_for<0, 2>([&](auto S) {
      constexpr int s = S.value;
      mma32(acc[0][0], w2f.af[10 % 3][s][0], hf[10][s][0]); mma32(acc[1][0], w2f.af[10 % 3][s][1], hf[10][s][0]);
      mma32(acc[0][1], w2f.af[10 % 3][s][0], hf[10][s][1]); mma32(acc[1][1], w2f.af[10 % 3][s][1], hf[10][s][1]);
    });
    MFMA_DRAIN();
  }
  MFMA_DRAIN();

  BAR_LDS();
  static_for<1, 4>([&](auto R) {
    constexpr int r = R.value;
    const f16v& a = acc[r >> 1][r & 1];
    const unsigned o = ((w ^ r) * 3 + (r - 1)) * 4096 + l * 16;
#pragma unroll
    for (int k = 0; k < 4; ++k) *lds_at<f4>(o + k * 1024) = f4{a[4 * k], a[4 * k + 1], a[4 * k + 2], a[4 * k + 3]};
  });
  asm volatile("s_waitcnt vmcnt(0)"
               : "+v"(av[0][0]), "+v"(av[0][1]), "+v"(av[1][0]), "+v"(av[1][1]), "+v"(av[2][0]), "+v"(av[2][1]), "+v"(av[3][0]), "+v"(av[3][1])::"memory");
  if (waita && !aok) {
    poll_flags(p.ws + WS_AFLAG + (nt + 8 * (l & 31)) * 8, l < 32, tag);
    const auto rs = __builtin_amdgcn_make_buffer_rsrc((void*)p.ws, 0, WS_END, 0x00020000);
#pragma unroll
    for (int i = 0; i < 8; ++i) av[i >> 1][i & 1] = __builtin_amdgcn_raw_buffer_load_b32(rs, ao + (4 * (i >> 1) + (i & 1)) * AT_STRIDE, 0, 16);
  }
  f4 y[4];
#pragma unroll
  for (int k = 0; k < 4; ++k) {
    const unsigned b0 = bv[k][0], b1 = bv[k][1], a0 = av[k][0], a1 = av[k][1];
    y[k] = f4{acc[0][0][4 * k] + (bf_lo(b0) + bf_lo(a0)), acc[0][0][4 * k + 1] + (bf_hi(b0) + bf_hi(a0)),
              acc[0][0][4 * k + 2] + (bf_lo(b1) + bf_lo(a1)), acc[0][0][4 * k + 3] + (bf_hi(b1) + bf_hi(a1))};
  }
  BAR_LDS();
  constexpr unsigned G_FS = 49152;
  {
    f4 pr[3][4];
#pragma unroll
    for (int si = 0; si < 3; ++si)
#pragma unroll
      for (int k = 0; k < 4; ++k) pr[si][k] = *lds_at<f4>((w * 3 + si) * 4096 + k * 1024 + l * 16);
    SCHED_FENCE();
#pragma unroll
    for (int k = 0; k < 4; ++k)
      *lds_at<f4>(G_FS + (32 * ftb + tok) * 272 + (32 * fcb + 8 * k + 4 * h) * 4) = (y[k] + pr[0][k]) + (pr[1][k] + pr[2][k]);
  }
  float xf0[8], xf1[8];
#pragma unroll
  for (int k = 0; k < 4; ++k) { xf0[2 * k] = bf_lo(xv0[k]); xf0[2 * k + 1] = bf_hi(xv0[k]); xf1[2 * k] = bf_lo(xv1[k]); xf1[2 * k + 1] = bf_hi(xv1[k]); }
  BAR_LDS();
  {
    const unsigned o = G_FS + ft * 272 + fc * 32;
    const f4 a0 = *lds_at<f4>(o), a1 = *lds_at<f4>(o + 16), b0 = *lds_at<f4>(o + 32 * 272), b1 = *lds_at<f4>(o + 32 * 272 + 16);
    SCHED_FENCE();
    u4 o0, o1;
    o0[0] = pk_bf(xf0[0] + a0[0], xf0[1] + a0[1]); o0[1] = pk_bf(xf0[2] + a0[2], xf0[3] + a0[3]);
    o0[2] = pk_bf(xf0[4] + a1[0], xf0[5] + a1[1]); o0[3] = pk_bf(xf0[6] + a1[2], xf0[7] + a1[3]);
    o1[0] = pk_bf(xf1[0] + b0[0], xf1[1] + b0[1]); o1[1] = pk_bf(xf1[2] + b0[2], xf1[3] + b0[3]);
    o1[2] = pk_bf(xf1[4] + b1[0], xf1[5] + b1[1]); o1[3] = pk_bf(xf1[6] + b1[2], xf1[7] + b1[3]);
    const auto ro = __builtin_amdgcn_make_buffer_rsrc((void*)p.out, 0, SEQ * DM * 2, 0x00020000);
    __builtin_amdgcn_raw_buffer_store_b128(o0, ro, go, 0, 0);
    __builtin_amdgcn_raw_buffer_store_b128(o1, ro, go + 32 * 1024, 0, 0);
  }
}

template <int MODE>
__global__ void __launch_bounds__(256, 2) fused_kernel(Params p) {
  const int b = blockIdx.x;
  constexpr bool fused = (MODE & 16) != 0;
  {
    u64 pc;
    int len;
    asm volatile("s_getpc_b64 %0\nS4D_TEXT_BEGIN:\n\ts_mov_b32 %1, S4D_TEXT_END-S4D_TEXT_BEGIN" : "=s"(pc), "=s"(len));
    const int off = (int)threadIdx.x * 128;
    if (b < 8) touch((const char*)pc, off < len ? (unsigned)off : 0u, lds_base() + S_SINK);
  }
  u64 tag = 0;
  if (b >= 256) {
    const int gb = b - 256;
    __builtin_amdgcn_s_setprio(PRIO_GEMM1);
    W2Dma w2d;
    W2Frag w2f;
    if constexpr ((MODE & 2) != 0) g_gemm1<fused, (MODE & 8) != 0>(p, gb, tag, w2d, w2f);
    if constexpr ((MODE & 8) != 0) g_gemm2<fused, fused && !(MODE & 32), (MODE & 2) != 0>(p, gb, tag, w2d, w2f);
  } else {
    const int sb = b & 255;
    if constexpr ((MODE & 5) != 0) s_role<fused, (MODE & 1) != 0, (MODE & 4) != 0>(p, sb, tag);
    if (fused && sb == 0) {
      const int tid = threadIdx.x;
      poll_flags(p.ws + WS_XNFLAG + tid * 8, true, tag);
      poll_flags(p.ws + WS_HFLAG + tid * 8, true, tag);
      BAR_LDS();
      if (tid == 0) st_flag(p.ws + WS_EPOCH, tag);
    }
  }
  asm volatile("S4D_TEXT_END:");
}

#if defined(__HIP_DEVICE_COMPILE__)
#pragma clang attribute pop
#endif
}

extern "C" void kernel_launch(void* const* d_in, const int* in_sizes, int n_in, void* d_out, int out_size,
                              void* d_ws, size_t ws_size, hipStream_t stream) {
  (void)in_sizes; (void)n_in; (void)out_size; (void)ws_size;
  Params p;
  p.x = (const char*)d_in[0];
  p.logA = (const unsigned short*)d_in[1];
  p.Aim = (const unsigned short*)d_in[2];
  p.Cre = (const unsigned short*)d_in[3];
  p.Cim = (const unsigned short*)d_in[4];
  p.logdt = (const unsigned short*)d_in[5];
  p.dskip = (const unsigned short*)d_in[6];
  p.w1 = (const char*)d_in[8];
  p.b1 = (const char*)d_in[9];
  p.w2 = (const char*)d_in[10];
  p.b2 = (const char*)d_in[11];
  p.out = (char*)d_out;
  p.ws = (char*)d_ws;
#define LAUNCH(M) do { (void)hipFuncSetAttribute((const void*)fused_kernel<M>, hipFuncAttributeMaxDynamicSharedMemorySize, LDS_BYTES); \
    fused_kernel<M><<<512, 256, LDS_BYTES, stream>>>(p); } while (0)
  LAUNCH(31);
}
```
